```python
import torch

def _softmax(x, axis=-1):
    return torch.softmax(x, dim=axis)
def _gelu(x):
    return torch.nn.functional.gelu(x, approximate="tanh")
def _silu(x):
    return torch.nn.functional.silu(x)
def _sigmoid(x):
    return torch.sigmoid(x)
def _layernorm(x, axis=-1, eps=1e-5):
    m = x.mean(dim=axis, keepdim=True)
    v = x.var(dim=axis, keepdim=True, unbiased=False)
    return (x - m) / torch.sqrt(v + eps)
def _scatter_add(tgt, idx, src, axis=0):
    return tgt.clone().index_add_(axis, idx, src)
def _segment_sum(src, seg, n):
    out = torch.zeros((n,) + src.shape[1:], dtype=src.dtype)
    return out.index_add_(0, seg, src)
def _pad(x, pw):
    flat = tuple(v for pair in reversed(pw) for v in pair)
    return torch.nn.functional.pad(x, flat)

kernel_name = 'standalone__spectral_fnet__prompt'

def setup_inputs():

    _w = (lambda a: (lambda t: t.to(torch.bfloat16) if t.is_floating_point() else t)(torch.as_tensor(a)))
    B, S, D = 1, 2048, 576
    ns = torch.arange(S)[:, None] * torch.arange(S)[None, :]
    nd = torch.arange(D)[:, None] * torch.arange(D)[None, :]
    Ws_re = torch.cos(-2.0 * torch.pi * ns / S).to(float)
    Ws_im = torch.sin(-2.0 * torch.pi * ns / S).to(float)
    Wd_re = torch.cos(-2.0 * torch.pi * nd / D).to(float)
    Wd_im = torch.sin(-2.0 * torch.pi * nd / D).to(float)
    return {
        "x": _w((lambda *s: torch.randn(*s, dtype=torch.bfloat16) * 0.05)(B, S, D)),
        "dft_seq_re": _w(Ws_re),
        "dft_seq_im": _w(Ws_im),
        "dft_hid_re": _w(Wd_re),
        "dft_hid_im": _w(Wd_im),
    }

def reference(x, dft_seq_re, dft_seq_im, dft_hid_re, dft_hid_im):
    a = torch.einsum("bsd,ed->bse", x, dft_hid_re)
    b = torch.einsum("bsd,ed->bse", x, dft_hid_im)
    y = torch.einsum("ts,bse->bte", dft_seq_re, a) - torch.einsum("ts,bse->bte", dft_seq_im, b)
    return y
```

```cpp
#include <hip/hip_runtime.h>
#include <hip/hip_bf16.h>
typedef __attribute__((ext_vector_type(8))) short bf16x8;
typedef __attribute__((ext_vector_type(4))) float f32x4;
typedef unsigned short u16;
constexpr int S=2048, D=576;

__device__ __forceinline__ u16 f2bf(float f){ __hip_bfloat16 h = __float2bfloat16(f); return *(u16*)&h; }

__global__ void k1(const u16* x, const u16* wre, const u16* wim, u16* abT){
  int wave = (blockIdx.x*blockDim.x+threadIdx.x)>>6; int lane=threadIdx.x&63;
  int tn = wave % 72, tm = wave / 72;
  const u16* w = tn<36 ? wre + (size_t)tn*16*D : wim + (size_t)(tn-36)*16*D;
  const u16* xa = x + (size_t)tm*16*D;
  f32x4 acc={0,0,0,0};
  for(int k=0;k<D;k+=32){
    bf16x8 a=*(const bf16x8*)(xa+(lane&15)*D+k+(lane>>4)*8);
    bf16x8 b=*(const bf16x8*)(w+(lane&15)*D+k+(lane>>4)*8);
    acc=__builtin_amdgcn_mfma_f32_16x16x32_bf16(a,b,acc,0,0,0);
  }
  float sg = tn<36?1.f:-1.f;
  int e = tn*16+(lane&15);
  for(int r=0;r<4;r++){ int s=tm*16+(lane>>4)*4+r; abT[(size_t)e*S+s]=f2bf(sg*acc[r]); }
}
__global__ void k2(const u16* sre, const u16* sim, const u16* abT, u16* y){
  int wave = (blockIdx.x*blockDim.x+threadIdx.x)>>6; int lane=threadIdx.x&63;
  int tn = wave % 36, tm = wave / 36;
  f32x4 acc={0,0,0,0};
  for(int h=0;h<2;h++){
    const u16* A=(h?sim:sre)+(size_t)tm*16*S;
    const u16* B=abT+(size_t)(h*576+tn*16)*S;
    for(int k=0;k<S;k+=32){
      bf16x8 a=*(const bf16x8*)(A+(lane&15)*S+k+(lane>>4)*8);
      bf16x8 b=*(const bf16x8*)(B+(lane&15)*S+k+(lane>>4)*8);
      acc=__builtin_amdgcn_mfma_f32_16x16x32_bf16(a,b,acc,0,0,0);
    }
  }
  for(int r=0;r<4;r++){ int t=tm*16+(lane>>4)*4+r; y[(size_t)t*D+tn*16+(lane&15)]=f2bf(acc[r]); }
}
extern "C" void kernel_launch(void* const* d_in, const int* in_sizes, int n_in,
                              void* d_out, int out_size, void* d_ws, size_t ws_size,
                              hipStream_t stream) {
  const u16* x=(const u16*)d_in[0]; const u16* sre=(const u16*)d_in[1]; const u16* sim=(const u16*)d_in[2];
  const u16* wre=(const u16*)d_in[3]; const u16* wim=(const u16*)d_in[4];
  u16* abT=(u16*)d_ws;
  k1<<<128*72/4,256,0,stream>>>(x,wre,wim,abT);
  k2<<<128*36/4,256,0,stream>>>(sre,sim,abT,(u16*)d_out);
}
```

```cpp
#include <hip/hip_runtime.h>
#include <hip/hip_bf16.h>
typedef __attribute__((ext_vector_type(8))) short bf16x8;
typedef __attribute__((ext_vector_type(4))) float f32x4;
typedef unsigned short u16;
constexpr int S=2048, D=576;

__device__ __forceinline__ u16 f2bf(float f){ __hip_bfloat16 h = __float2bfloat16(f); return *(u16*)&h; }

__global__ void k1(const u16* x, const u16* wre, const u16* wim, u16* abT){
  int wave = (blockIdx.x*blockDim.x+threadIdx.x)>>6; int lane=threadIdx.x&63;
  int tn = wave % 72, tm = wave / 72;
  const u16* w = tn<36 ? wre + (size_t)tn*16*D : wim + (size_t)(tn-36)*16*D;
  const u16* xa = x + (size_t)tm*16*D;
  f32x4 acc={0,0,0,0};
  for(int k=0;k<D;k+=32){
    bf16x8 a=*(const bf16x8*)(xa+(lane&15)*D+k+(lane>>4)*8);
    bf16x8 b=*(const bf16x8*)(w+(lane&15)*D+k+(lane>>4)*8);
    acc=__builtin_amdgcn_mfma_f32_16x16x32_bf16(a,b,acc,0,0,0);
  }
  float sg = tn<36?1.f:-1.f;
  int e = tn*16+(lane&15);
  for(int r=0;r<4;r++){ int s=tm*16+(lane>>4)*4+r; abT[(size_t)e*S+s]=f2bf(sg*acc[r]); }
}
__global__ void k2(const u16* sre, const u16* sim, const u16* abT, u16* y){
  int wave = (blockIdx.x*blockDim.x+threadIdx.x)>>6; int lane=threadIdx.x&63;
  int tn = wave % 36, tm = wave / 36;
  f32x4 acc={0,0,0,0};
  for(int h=0;h<2;h++){
    const u16* A=(h?sim:sre)+(size_t)tm*16*S;
    const u16* B=abT+(size_t)(h*576+tn*16)*S;
    for(int k=0;k<S;k+=32){
      bf16x8 a=*(const bf16x8*)(A+(lane&15)*S+k+(lane>>4)*8);
      bf16x8 b=*(const bf16x8*)(B+(lane&15)*S+k+(lane>>4)*8);
      acc=__builtin_amdgcn_mfma_f32_16x16x32_bf16(a,b,acc,0,0,0);
    }
  }
  for(int r=0;r<4;r++){ int t=tm*16+(lane>>4)*4+r; y[(size_t)t*D+tn*16+(lane&15)]=f2bf(acc[r]); }
}

typedef __attribute__((ext_vector_type(4))) unsigned u32x4;
__global__ __launch_bounds__(256,1) void probe(const u16* x, const u16* sre, const u16* sim, const u16* wre, const u16* wim, unsigned* ws){
  unsigned long long t0=__builtin_amdgcn_s_memrealtime();
  asm volatile("s_waitcnt lgkmcnt(0)":::"memory");
  __builtin_amdgcn_sched_barrier(0);
  int wg=blockIdx.x, tid=threadIdx.x;
  asm volatile("" :: "s"(x),"s"(sre),"s"(sim),"s"(wre),"s"(wim),"s"(ws));
  asm volatile("s_waitcnt vmcnt(0) lgkmcnt(0)":::"memory");
  __builtin_amdgcn_sched_barrier(0);
  unsigned long long t1=__builtin_amdgcn_s_memrealtime();
  unsigned long long until=t0+(t1-t0)*50ull;
  if(until>t0+200000ull) until=t0+200000ull;
  while(__builtin_amdgcn_s_memrealtime()<until) __builtin_amdgcn_s_sleep(2);
}
extern "C" void kernel_launch(void* const* d_in, const int* in_sizes, int n_in,
                              void* d_out, int out_size, void* d_ws, size_t ws_size,
                              hipStream_t stream) {
  const u16* x=(const u16*)d_in[0]; const u16* sre=(const u16*)d_in[1]; const u16* sim=(const u16*)d_in[2];
  const u16* wre=(const u16*)d_in[3]; const u16* wim=(const u16*)d_in[4];
  u16* abT=(u16*)d_ws;
  probe<<<256,256,0,stream>>>(x,sre,sim,wre,wim,(unsigned*)d_ws+(16<<20));
  k1<<<128*72/4,256,0,stream>>>(x,wre,wim,abT);
  k2<<<128*36/4,256,0,stream>>>(sre,sim,abT,(u16*)d_out);
}
```

```cpp
#include <hip/hip_runtime.h>

__global__ __launch_bounds__(256, 1) void fnet_fused(const void* x, const void* sre, const void* sim, const void* wre, const void* wim, void* out, void* ws) {
  __shared__ char lds[163840];
  asm volatile(
    "LBEG_%=:\n"
    "s_load_dwordx8 s[8:15], s[4:5], 0\n"
    "s_load_dwordx4 s[16:19], s[4:5], 32\n"
    "s_load_dwordx2 s[20:21], s[4:5], 48\n"
    "v_lshrrev_b32 v2, 6, v0\n"
    "v_and_b32 v1, 63, v0\n"
    "v_readfirstlane_b32 s22, v2\n"
    "s_bfe_u32 s25, s6, 0x20001\n"
    "s_and_b32 s28, s6, 1\n"
    "s_lshl_b32 s28, s28, 3\n"
    "s_lshr_b32 s7, s6, 5\n"
    "s_add_u32 s23, s28, s7\n"
    "s_bfe_u32 s24, s6, 0x20003\n"
    "v_lshrrev_b32 v3, 3, v1\n"
    "v_lshrrev_b32 v4, 4, v1\n"
    "v_and_b32 v5, 7, v1\n"
    "v_xor_b32 v5, v5, v4\n"
    "v_lshlrev_b32 v5, 4, v5\n"
    "v_and_b32 v6, 3, v3\n"
    "v_lshrrev_b32 v7, 5, v1\n"
    "v_lshl_add_u32 v6, v7, 3, v6\n"
    "s_movk_i32 s30, 1152\n"
    "v_mad_u32_u24 v7, v6, s30, v5\n"
    "v_xor_b32 v8, 64, v5\n"
    "v_mad_u32_u24 v9, v6, s30, v8\n"
    "s_add_u32 s28, s23, s24\n"
    "s_and_b32 s28, s28, 1\n"
    "s_and_b32 s31, s25, 1\n"
    "s_lshr_b32 s35, s23, 1\n"
    "s_lshl_b32 s26, s31, 3\n"
    "s_add_u32 s26, s26, s35\n"
    "s_lshl_b32 s26, s26, 2\n"
    "s_add_u32 s26, s26, s22\n"
    "s_mul_i32 s26, s26, 36864\n"
    "v_add_u32 v10, s26, v7\n"
    "s_add_u32 s27, s26, 17408\n"
    "v_add_u32 v11, s27, v9\n"
    "v_add_u32 v12, 2560, v10\n"
    "v_add_u32 v13, 2560, v11\n"
    "s_mul_i32 s34, s24, 147456\n"
    "s_add_u32 s26, s25, s22\n"
    "s_add_u32 s26, s26, 1\n"
    "s_and_b32 s26, s26, 3\n"
    "s_mul_i32 s26, s26, 36864\n"
    "s_add_u32 s26, s26, s34\n"
    "v_add_u32 v14, s26, v7\n"
    "s_add_u32 s27, s26, 17408\n"
    "v_add_u32 v15, s27, v9\n"
    "v_add_u32 v16, 2560, v14\n"
    "v_add_u32 v17, 2560, v15\n"
    "s_mov_b32 s38, 2359296\n"
    "s_mov_b32 s39, 0x20000\n"
    "s_mov_b32 s42, 663552\n"
    "s_mov_b32 s43, 0x20000\n"
    "s_lshl_b32 s59, s22, 12\n"
    "s_add_u32 s60, s24, s35\n"
    "s_and_b32 s60, s60, 3\n"
    "s_add_u32 s60, s60, 1\n"
    "s_mul_i32 s60, s60, s28\n"
    "s_lshl_b32 s60, s60, 7\n"
    "s_mov_b32 s61, s60\n"
    "s_bitcmp0_b32 s25, 1\n"
    "s_waitcnt lgkmcnt(0)\n"
    "s_mov_b64 s[36:37], s[8:9]\n"
    "s_cselect_b64 s[40:41], s[14:15], s[16:17]\n"
    "s_cselect_b64 s[44:45], s[10:11], s[12:13]\n"
    "s_cselect_b32 s58, 0, 0x80008000\n"
    "s_lshr_b32 s26, s60, 7\n"
    "s_sub_u32 s26, s26, 1\n"
    "s_cbranch_scc1 L1_%=\n"
    "L2_%=:\n"
    "s_sleep 4\n"
    "s_sub_u32 s26, s26, 1\n"
    "s_cbranch_scc0 L2_%=\n"
    "L1_%=:\n"
    "s_add_u32 m0, s59, 65536\n"
    "s_nop 0\n"
    "buffer_load_dwordx4 v10, s[36:39], s60 offen offset:0 lds\n"
    "buffer_load_dwordx4 v11, s[36:39], s60 offen offset:1024 lds\n"
    "buffer_load_dwordx4 v12, s[36:39], s60 offen offset:2048 lds\n"
    "buffer_load_dwordx4 v13, s[36:39], s60 offen offset:3072 lds\n"
    "s_add_u32 m0, s59, 0\n"
    "s_nop 0\n"
    "buffer_load_dwordx4 v14, s[40:43], s61 offen offset:0 lds\n"
    "buffer_load_dwordx4 v15, s[40:43], s61 offen offset:1024 lds\n"
    "buffer_load_dwordx4 v16, s[40:43], s61 offen offset:2048 lds\n"
    "buffer_load_dwordx4 v17, s[40:43], s61 offen offset:3072 lds\n"
    "s_cmp_eq_u32 s28, 0\n"
    "s_cbranch_scc1 L3_%=\n"
    "s_getpc_b64 s[64:65]\n"
    "LCP_%=:\n"
    "s_lshl_b32 s14, s22, 13\n"
    "v_lshlrev_b32 v35, 7, v1\n"
    "v_add_u32 v35, s14, v35\n"
    "v_min_u32 v35, LEND_%=-LCP_%=, v35\n"
    "global_load_dword v37, v35, s[64:65]\n"
    "L3_%=:\n"
    "s_cmp_eq_u32 s28, 0\n"
    "s_cbranch_scc0 L4_%=\n"
    "v_and_b32 v2, 15, v1\n"
    "v_lshrrev_b32 v6, 1, v2\n"
    "v_xor_b32 v6, v6, v4\n"
    "v_lshlrev_b32 v6, 4, v6\n"
    "v_lshl_add_u32 v24, v2, 7, v6\n"
    "v_xor_b32 v25, 64, v24\n"
    "s_add_u32 s56, s59, 65536\n"
    "v_add_u32 v28, s56, v24\n"
    "v_add_u32 v29, s56, v25\n"
    "s_and_b32 s27, s22, 1\n"
    "s_lshl_b32 s33, s22, 10\n"
    "s_mul_i32 s26, s27, 9216\n"
    "s_lshr_b32 s34, s34, 3\n"
    "s_add_u32 s26, s26, s34\n"
    "s_add_u32 s26, s26, 589824\n"
    "s_lshr_b32 s29, s22, 1\n"
    "s_lshl_b32 s29, s29, 7\n"
    "s_add_u32 s26, s26, s29\n"
    "s_lshl_b32 s27, s27, 6\n"
    "v_xor_b32 v18, s27, v5\n"
    "v_mad_u32_u24 v18, v3, s30, v18\n"
    "v_add_u32 v18, s26, v18\n"
    "v_subrev_u32 v36, s29, v18\n"
    "s_add_u32 m0, s33, 147456\n"
    "s_nop 0\n"
    "buffer_load_dwordx4 v18, s[40:43], s61 offen offset:0 lds\n"
    "v_add_u32 v26, 0x20000, v24\n"
    "v_add_u32 v27, 0x20000, v25\n"
    "s_add_u32 m0, s59, 81920\n"
    "s_movk_i32 s60, 128\n"
    "buffer_load_dwordx4 v10, s[36:39], s60 offen offset:0 lds\n"
    "buffer_load_dwordx4 v11, s[36:39], s60 offen offset:1024 lds\n"
    "buffer_load_dwordx4 v12, s[36:39], s60 offen offset:2048 lds\n"
    "buffer_load_dwordx4 v13, s[36:39], s60 offen offset:3072 lds\n"
    "s_add_u32 m0, s59, 16384\n"
    "s_movk_i32 s61, 128\n"
    "buffer_load_dwordx4 v14, s[40:43], s61 offen offset:0 lds\n"
    "buffer_load_dwordx4 v15, s[40:43], s61 offen offset:1024 lds\n"
    "buffer_load_dwordx4 v16, s[40:43], s61 offen offset:2048 lds\n"
    "buffer_load_dwordx4 v17, s[40:43], s61 offen offset:3072 lds\n"
    "s_waitcnt vmcnt(13)\n"
    "ds_read_b128 a[112:115], v28 offset:0\n"
    "ds_read_b128 a[116:119], v28 offset:2048\n"
    "ds_read_b128 a[120:123], v29 offset:0\n"
    "ds_read_b128 a[124:127], v29 offset:2048\n"
    "s_waitcnt vmcnt(8)\n"
    "s_barrier\n"
    "ds_read_b128 v[136:139], v24 offset:0\n"
    "ds_read_b128 v[140:143], v24 offset:2048\n"
    "ds_read_b128 v[144:147], v24 offset:4096\n"
    "ds_read_b128 v[148:151], v24 offset:6144\n"
    "ds_read_b128 v[152:155], v24 offset:8192\n"
    "ds_read_b128 v[156:159], v24 offset:10240\n"
    "ds_read_b128 v[160:163], v26 offset:16384\n"
    "ds_read_b128 v[164:167], v24 offset:12288\n"
    "s_waitcnt lgkmcnt(5)\n"
    "v_mfma_f32_16x16x32_bf16 v[64:67], a[112:115], v[136:139], 0\n"
    "s_add_u32 m0, s59, 65536\n"
    "s_movk_i32 s60, 256\n"
    "s_mov_b32 s46, 8388608\n"
    "v_mfma_f32_16x16x32_bf16 v[68:71], a[116:119], v[136:139], 0\n"
    "ds_read_b128 v[168:171], v24 offset:14336\n"
    "s_mov_b32 s47, 0x20000\n"
    "s_mov_b64 s[48:49], s[20:21]\n"
    "v_mfma_f32_16x16x32_bf16 v[72:75], a[112:115], v[140:143], 0\n"
    "buffer_load_dwordx4 v10, s[36:39], s60 offen offset:0 lds\n"
    "v_mfma_f32_16x16x32_bf16 v[76:79], a[116:119], v[140:143], 0\n"
    "ds_read_b128 v[172:175], v25 offset:0\n"
    "s_mov_b32 s50, 16777216\n"
    "s_mov_b32 s51, 0x20000\n"
    "v_mfma_f32_16x16x32_bf16 v[80:83], a[112:115], v[144:147], 0\n"
    "buffer_load_dwordx4 v11, s[36:39], s60 offen offset:1024 lds\n"
    "v_mfma_f32_16x16x32_bf16 v[84:87], a[116:119], v[144:147], 0\n"
    "ds_read_b128 v[176:179], v25 offset:2048\n"
    "s_mov_b64 s[52:53], s[18:19]\n"
    "s_waitcnt lgkmcnt(5)\n"
    "v_mfma_f32_16x16x32_bf16 v[88:91], a[112:115], v[148:151], 0\n"
    "buffer_load_dwordx4 v12, s[36:39], s60 offen offset:2048 lds\n"
    "v_mfma_f32_16x16x32_bf16 v[92:95], a[116:119], v[148:151], 0\n"
    "ds_read_b128 v[180:183], v25 offset:4096\n"
    "s_mov_b32 s54, 2359296\n"
    "s_mov_b32 s55, 0x20000\n"
    "v_mfma_f32_16x16x32_bf16 v[96:99], a[112:115], v[152:155], 0\n"
    "buffer_load_dwordx4 v13, s[36:39], s60 offen offset:3072 lds\n"
    "v_mfma_f32_16x16x32_bf16 v[100:103], a[116:119], v[152:155], 0\n"
    "ds_read_b128 v[136:139], v25 offset:6144\n"
    "s_add_u32 m0, s59, 32768\n"
    "s_movk_i32 s61, 256\n"
    "v_mfma_f32_16x16x32_bf16 v[104:107], a[112:115], v[156:159], 0\n"
    "buffer_load_dwordx4 v14, s[40:43], s61 offen offset:0 lds\n"
    "v_mfma_f32_16x16x32_bf16 v[108:111], a[116:119], v[156:159], 0\n"
    "ds_read_b128 v[140:143], v25 offset:8192\n"
    "s_lshl_b32 s26, s23, 2\n"
    "s_waitcnt lgkmcnt(5)\n"
    "v_mfma_f32_16x16x32_bf16 v[112:115], a[112:115], v[160:163], 0\n"
    "buffer_load_dwordx4 v15, s[40:43], s61 offen offset:1024 lds\n"
    "v_mfma_f32_16x16x32_bf16 v[116:119], a[116:119], v[160:163], 0\n"
    "ds_read_b128 v[144:147], v25 offset:10240\n"
    "s_add_u32 s34, s26, s24\n"
    "s_add_u32 s26, s26, s22\n"
    "v_mfma_f32_16x16x32_bf16 v[120:123], a[112:115], v[164:167], 0\n"
    "buffer_load_dwordx4 v16, s[40:43], s61 offen offset:2048 lds\n"
    "v_mfma_f32_16x16x32_bf16 v[124:127], a[116:119], v[164:167], 0\n"
    "ds_read_b128 v[148:151], v27 offset:16384\n"
    "s_mul_i32 s18, s26, 36864\n"
    "s_lshl_b32 s26, s26, 17\n"
    "v_mfma_f32_16x16x32_bf16 v[128:131], a[112:115], v[168:171], 0\n"
    "buffer_load_dwordx4 v17, s[40:43], s61 offen offset:3072 lds\n"
    "v_mfma_f32_16x16x32_bf16 v[132:135], a[116:119], v[168:171], 0\n"
    "ds_read_b128 v[152:155], v25 offset:12288\n"
    "s_add_u32 m0, s33, 151552\n"
    "s_waitcnt lgkmcnt(5)\n"
    "v_mfma_f32_16x16x32_bf16 v[64:67], a[120:123], v[172:175], v[64:67]\n"
    "s_movk_i32 s61, 256\n"
    "s_lshl_b32 s27, s31, 11\n"
    "s_add_u32 s26, s26, s27\n"
    "v_mfma_f32_16x16x32_bf16 v[68:71], a[124:127], v[172:175], v[68:71]\n"
    "ds_read_b128 v[156:159], v25 offset:14336\n"
    "v_lshlrev_b32 v6, 12, v3\n"
    "v_mfma_f32_16x16x32_bf16 v[72:75], a[120:123], v[176:179], v[72:75]\n"
    "s_waitcnt vmcnt(12)\n"
    "ds_read_b128 a[128:131], v28 offset:16384\n"
    "ds_read_b128 a[132:135], v28 offset:18432\n"
    "ds_read_b128 a[136:139], v29 offset:16384\n"
    "v_mfma_f32_16x16x32_bf16 v[76:79], a[124:127], v[176:179], v[76:79]\n"
    "s_waitcnt vmcnt(8)\n"
    "s_barrier\n"
    "ds_read_b128 v[160:163], v24 offset:16384\n"
    "v_mfma_f32_16x16x32_bf16 v[80:83], a[120:123], v[180:183], v[80:83]\n"
    "ds_read_b128 a[140:143], v29 offset:18432\n"
    "v_add_u32 v6, s26, v6\n"
    "v_mfma_f32_16x16x32_bf16 v[84:87], a[124:127], v[180:183], v[84:87]\n"
    "ds_read_b128 v[164:167], v24 offset:18432\n"
    "s_waitcnt lgkmcnt(9)\n"
    "v_mfma_f32_16x16x32_bf16 v[88:91], a[120:123], v[136:139], v[88:91]\n"
    "buffer_load_dwordx4 v18, s[40:43], s61 offen offset:0 lds\n"
    "v_mfma_f32_16x16x32_bf16 v[92:95], a[124:127], v[136:139], v[92:95]\n"
    "ds_read_b128 v[168:171], v24 offset:20480\n"
    "s_add_u32 m0, s59, 98304\n"
    "s_movk_i32 s60, 384\n"
    "v_mfma_f32_16x16x32_bf16 v[96:99], a[120:123], v[140:143], v[96:99]\n"
    "v_add_u32 v19, v6, v5\n"
    "v_add_u32 v20, v6, v8\n"
    "v_mfma_f32_16x16x32_bf16 v[100:103], a[124:127], v[140:143], v[100:103]\n"
    "ds_read_b128 v[172:175], v24 offset:22528\n"
    "v_add_u32 v21, 63488, v19\n"
    "v_mfma_f32_16x16x32_bf16 v[104:107], a[120:123], v[144:147], v[104:107]\n"
    "buffer_load_dwordx4 v10, s[36:39], s60 offen offset:0 lds\n"
    "v_mfma_f32_16x16x32_bf16 v[108:111], a[124:127], v[144:147], v[108:111]\n"
    "ds_read_b128 v[176:179], v24 offset:24576\n"
    "s_waitcnt lgkmcnt(9)\n"
    "v_mfma_f32_16x16x32_bf16 v[112:115], a[120:123], v[148:151], v[112:115]\n"
    "v_add_u32 v22, 95232, v20\n"
    "v_add_u32 v20, 31744, v20\n"
    "v_mfma_f32_16x16x32_bf16 v[116:119], a[124:127], v[148:151], v[116:119]\n"
    "ds_read_b128 v[180:183], v24 offset:26624\n"
    "s_lshl_b32 s26, s35, 1\n"
    "s_lshl_b32 s62, s26, 7\n"
    "v_mfma_f32_16x16x32_bf16 v[120:123], a[120:123], v[152:155], v[120:123]\n"
    "buffer_load_dwordx4 v11, s[36:39], s60 offen offset:1024 lds\n"
    "v_mfma_f32_16x16x32_bf16 v[124:127], a[124:127], v[152:155], v[124:127]\n"
    "ds_read_b128 v[136:139], v26 offset:18432\n"
    "v_mov_b32 v45, 0\n"
    "v_mfma_f32_16x16x32_bf16 v[128:131], a[120:123], v[156:159], v[128:131]\n"
    "s_lshl_b32 s19, s24, 8\n"
    "s_add_u32 s19, s19, 15335424\n"
    "s_lshl_b32 s69, s26, 1\n"
    "v_mfma_f32_16x16x32_bf16 v[132:135], a[124:127], v[156:159], v[132:135]\n"
    "ds_read_b128 v[140:143], v24 offset:28672\n"
    "s_add_u32 s69, s69, s22\n"
    "s_waitcnt lgkmcnt(5)\n"
    "v_mfma_f32_16x16x32_bf16 v[64:67], a[128:131], v[160:163], v[64:67]\n"
    "buffer_load_dwordx4 v12, s[36:39], s60 offen offset:2048 lds\n"
    "v_mfma_f32_16x16x32_bf16 v[68:71], a[132:135], v[160:163], v[68:71]\n"
    "ds_read_b128 v[144:147], v24 offset:30720\n"
    "s_add_u32 s56, s69, s22\n"
    "v_mfma_f32_16x16x32_bf16 v[72:75], a[128:131], v[164:167], v[72:75]\n"
    "v_lshlrev_b32 v23, 4, v1\n"
    "s_lshl_b32 s26, s25, 2\n"
    "v_mfma_f32_16x16x32_bf16 v[76:79], a[132:135], v[164:167], v[76:79]\n"
    "ds_read_b128 v[148:151], v25 offset:16384\n"
    "s_add_u32 s26, s26, s24\n"
    "s_mul_i32 s68, s26, 294912\n"
    "v_mfma_f32_16x16x32_bf16 v[80:83], a[128:131], v[168:171], v[80:83]\n"
    "buffer_load_dwordx4 v13, s[36:39], s60 offen offset:3072 lds\n"
    "v_mfma_f32_16x16x32_bf16 v[84:87], a[132:135], v[168:171], v[84:87]\n"
    "ds_read_b128 v[152:155], v25 offset:18432\n"
    "s_add_u32 m0, s59, 49152\n"
    "s_waitcnt lgkmcnt(5)\n"
    "v_mfma_f32_16x16x32_bf16 v[88:91], a[128:131], v[172:175], v[88:91]\n"
    "s_movk_i32 s61, 384\n"
    "s_lshl_b32 s31, s35, 2\n"
    "s_add_u32 s31, s31, s22\n"
    "v_mfma_f32_16x16x32_bf16 v[92:95], a[132:135], v[172:175], v[92:95]\n"
    "ds_read_b128 v[156:159], v25 offset:20480\n"
    "s_mul_i32 s27, s31, 4096\n"
    "s_add_u32 s27, s27, s68\n"
    "v_mfma_f32_16x16x32_bf16 v[96:99], a[128:131], v[176:179], v[96:99]\n"
    "buffer_load_dwordx4 v14, s[40:43], s61 offen offset:0 lds\n"
    "v_mfma_f32_16x16x32_bf16 v[100:103], a[132:135], v[176:179], v[100:103]\n"
    "ds_read_b128 v[160:163], v25 offset:22528\n"
    "s_add_u32 s8, s27, 0\n"
    "s_mul_i32 s27, s31, 3072\n"
    "v_mfma_f32_16x16x32_bf16 v[104:107], a[128:131], v[180:183], v[104:107]\n"
    "s_add_u32 s27, s27, s68\n"
    "s_add_u32 s9, s27, 131072\n"
    "s_mul_i32 s27, s31, 2048\n"
    "v_mfma_f32_16x16x32_bf16 v[108:111], a[132:135], v[180:183], v[108:111]\n"
    "ds_read_b128 v[164:167], v25 offset:24576\n"
    "s_add_u32 s27, s27, s68\n"
    "s_waitcnt lgkmcnt(5)\n"
    "v_mfma_f32_16x16x32_bf16 v[112:115], a[128:131], v[136:139], v[112:115]\n"
    "buffer_load_dwordx4 v15, s[40:43], s61 offen offset:1024 lds\n"
    "v_mfma_f32_16x16x32_bf16 v[116:119], a[132:135], v[136:139], v[116:119]\n"
    "ds_read_b128 v[168:171], v25 offset:26624\n"
    "s_add_u32 s10, s27, 229376\n"
    "v_mfma_f32_16x16x32_bf16 v[120:123], a[128:131], v[140:143], v[120:123]\n"
    "buffer_load_dwordx4 v16, s[40:43], s61 offen offset:2048 lds\n"
    "v_mfma_f32_16x16x32_bf16 v[124:127], a[132:135], v[140:143], v[124:127]\n"
    "ds_read_b128 v[172:175], v27 offset:18432\n"
    "v_add_u32 v33, s59, v23\n"
    "v_mfma_f32_16x16x32_bf16 v[128:131], a[128:131], v[144:147], v[128:131]\n"
    "buffer_load_dwordx4 v17, s[40:43], s61 offen offset:3072 lds\n"
    "v_mfma_f32_16x16x32_bf16 v[132:135], a[132:135], v[144:147], v[132:135]\n"
    "ds_read_b128 v[176:179], v25 offset:28672\n"
    "s_add_u32 m0, s59, 81920\n"
    "s_waitcnt lgkmcnt(5)\n"
    "v_mfma_f32_16x16x32_bf16 v[64:67], a[136:139], v[148:151], v[64:67]\n"
    "s_movk_i32 s60, 512\n"
    "v_add_u32 v34, 0x20000, v33\n"
    "v_mfma_f32_16x16x32_bf16 v[68:71], a[140:143], v[148:151], v[68:71]\n"
    "ds_read_b128 v[180:183], v25 offset:30720\n"
    "s_lshl_b32 s35, s26, 12\n"
    "s_add_u32 s35, s35, 15204352\n"
    "v_mfma_f32_16x16x32_bf16 v[72:75], a[136:139], v[152:155], v[72:75]\n"
    "s_waitcnt vmcnt(13)\n"
    "ds_read_b128 a[144:147], v28 offset:0\n"
    "ds_read_b128 a[148:151], v28 offset:2048\n"
    "ds_read_b128 a[152:155], v29 offset:0\n"
    "v_mfma_f32_16x16x32_bf16 v[76:79], a[140:143], v[152:155], v[76:79]\n"
    "s_waitcnt vmcnt(8)\n"
    "s_barrier\n"
    "ds_read_b128 v[136:139], v24 offset:32768\n"
    "v_mfma_f32_16x16x32_bf16 v[80:83], a[136:139], v[156:159], v[80:83]\n"
    "ds_read_b128 a[156:159], v29 offset:2048\n"
    "s_lshl_b32 s20, s23, 8\n"
    "s_add_u32 s20, s20, s35\n"
    "v_mfma_f32_16x16x32_bf16 v[84:87], a[140:143], v[156:159], v[84:87]\n"
    "ds_read_b128 v[140:143], v24 offset:34816\n"
    "s_waitcnt lgkmcnt(9)\n"
    "v_mfma_f32_16x16x32_bf16 v[88:91], a[136:139], v[160:163], v[88:91]\n"
    "buffer_load_dwordx4 v10, s[36:39], s60 offen offset:0 lds\n"
    "v_mfma_f32_16x16x32_bf16 v[92:95], a[140:143], v[160:163], v[92:95]\n"
    "ds_read_b128 v[144:147], v24 offset:36864\n"
    "v_lshlrev_b32 v42, 8, v2\n"
    "v_mfma_f32_16x16x32_bf16 v[96:99], a[136:139], v[164:167], v[96:99]\n"
    "v_add_u32 v42, s35, v42\n"
    "v_mov_b32 v9, s19\n"
    "v_mfma_f32_16x16x32_bf16 v[100:103], a[140:143], v[164:167], v[100:103]\n"
    "ds_read_b128 v[148:151], v24 offset:38912\n"
    "v_mfma_f32_16x16x32_bf16 v[104:107], a[136:139], v[168:171], v[104:107]\n"
    "buffer_load_dwordx4 v11, s[36:39], s60 offen offset:1024 lds\n"
    "v_mfma_f32_16x16x32_bf16 v[108:111], a[140:143], v[168:171], v[108:111]\n"
    "ds_read_b128 v[152:155], v24 offset:40960\n"
    "s_waitcnt lgkmcnt(9)\n"
    "v_mfma_f32_16x16x32_bf16 v[112:115], a[136:139], v[172:175], v[112:115]\n"
    "s_cmp_eq_u32 s22, 0\n"
    "s_cselect_b64 vcc, -1, 0\n"
    "v_cndmask_b32 v42, v9, v42, vcc\n"
    "v_mfma_f32_16x16x32_bf16 v[116:119], a[140:143], v[172:175], v[116:119]\n"
    "ds_read_b128 v[156:159], v24 offset:43008\n"
    "v_mov_b32 v30, v23\n"
    "v_mfma_f32_16x16x32_bf16 v[120:123], a[136:139], v[176:179], v[120:123]\n"
    "buffer_load_dwordx4 v12, s[36:39], s60 offen offset:2048 lds\n"
    "v_mfma_f32_16x16x32_bf16 v[124:127], a[140:143], v[176:179], v[124:127]\n"
    "ds_read_b128 v[160:163], v26 offset:20480\n"
    "v_add_u32 v31, 0x10000, v23\n"
    "v_mfma_f32_16x16x32_bf16 v[128:131], a[136:139], v[180:183], v[128:131]\n"
    "v_add_u32 v32, 0x20000, v23\n"
    "v_and_b32 v9, 3, v1\n"
    "v_mfma_f32_16x16x32_bf16 v[132:135], a[140:143], v[180:183], v[132:135]\n"
    "ds_read_b128 v[164:167], v24 offset:45056\n"
    "s_waitcnt lgkmcnt(5)\n"
    "v_mfma_f32_16x16x32_bf16 v[64:67], a[144:147], v[136:139], v[64:67]\n"
    "buffer_load_dwordx4 v13, s[36:39], s60 offen offset:3072 lds\n"
    "v_mfma_f32_16x16x32_bf16 v[68:71], a[148:151], v[136:139], v[68:71]\n"
    "ds_read_b128 v[168:171], v24 offset:47104\n"
    "s_add_u32 m0, s59, 0\n"
    "s_movk_i32 s61, 512\n"
    "v_mfma_f32_16x16x32_bf16 v[72:75], a[144:147], v[140:143], v[72:75]\n"
    "v_min_u32 v9, 2, v9\n"
    "s_add_u32 s26, s25, 1\n"
    "v_mfma_f32_16x16x32_bf16 v[76:79], a[148:151], v[140:143], v[76:79]\n"
    "ds_read_b128 v[172:175], v25 offset:32768\n"
    "v_add_u32 v9, s26, v9\n"
    "v_mfma_f32_16x16x32_bf16 v[80:83], a[144:147], v[144:147], v[80:83]\n"
    "buffer_load_dwordx4 v14, s[40:43], s61 offen offset:0 lds\n"
    "v_mfma_f32_16x16x32_bf16 v[84:87], a[148:151], v[144:147], v[84:87]\n"
    "ds_read_b128 v[176:179], v25 offset:34816\n"
    "s_waitcnt lgkmcnt(5)\n"
    "v_mfma_f32_16x16x32_bf16 v[88:91], a[144:147], v[148:151], v[88:91]\n"
    "v_and_b32 v9, 3, v9\n"
    "v_lshlrev_b32 v44, 8, v9\n"
    "v_mfma_f32_16x16x32_bf16 v[92:95], a[148:151], v[148:151], v[92:95]\n"
    "ds_read_b128 v[180:183], v25 offset:36864\n"
    "v_mov_b32 v60, -1\n"
    "v_mfma_f32_16x16x32_bf16 v[96:99], a[144:147], v[152:155], v[96:99]\n"
    "buffer_load_dwordx4 v15, s[40:43], s61 offen offset:1024 lds\n"
    "v_mfma_f32_16x16x32_bf16 v[100:103], a[148:151], v[152:155], v[100:103]\n"
    "ds_read_b128 v[136:139], v25 offset:38912\n"
    "v_mov_b32 v61, -1\n"
    "v_mfma_f32_16x16x32_bf16 v[104:107], a[144:147], v[156:159], v[104:107]\n"
    "v_mov_b32 v62, -1\n"
    "v_mov_b32 v63, -1\n"
    "v_mfma_f32_16x16x32_bf16 v[108:111], a[148:151], v[156:159], v[108:111]\n"
    "ds_read_b128 v[140:143], v25 offset:40960\n"
    "s_lshl_b32 s7, s34, 2\n"
    "s_waitcnt lgkmcnt(5)\n"
    "v_mfma_f32_16x16x32_bf16 v[112:115], a[144:147], v[160:163], v[112:115]\n"
    "buffer_load_dwordx4 v16, s[40:43], s61 offen offset:2048 lds\n"
    "v_mfma_f32_16x16x32_bf16 v[116:119], a[148:151], v[160:163], v[116:119]\n"
    "ds_read_b128 v[144:147], v25 offset:43008\n"
    "s_add_u32 s26, s25, 1\n"
    "s_and_b32 s26, s26, 3\n"
    "v_mfma_f32_16x16x32_bf16 v[120:123], a[144:147], v[164:167], v[120:123]\n"
    "buffer_load_dwordx4 v17, s[40:43], s61 offen offset:3072 lds\n"
    "v_mfma_f32_16x16x32_bf16 v[124:127], a[148:151], v[164:167], v[124:127]\n"
    "ds_read_b128 v[148:151], v27 offset:20480\n"
    "s_add_u32 m0, s33, 155648\n"
    "s_movk_i32 s61, 512\n"
    "v_mfma_f32_16x16x32_bf16 v[128:131], a[144:147], v[168:171], v[128:131]\n"
    "buffer_load_dwordx4 v18, s[40:43], s61 offen offset:0 lds\n"
    "v_mfma_f32_16x16x32_bf16 v[132:135], a[148:151], v[168:171], v[132:135]\n"
    "ds_read_b128 v[152:155], v25 offset:45056\n"
    "s_add_u32 m0, s59, 65536\n"
    "s_waitcnt lgkmcnt(5)\n"
    "v_mfma_f32_16x16x32_bf16 v[64:67], a[152:155], v[172:175], v[64:67]\n"
    "s_movk_i32 s60, 640\n"
    "s_add_u32 s26, s26, s7\n"
    "s_lshl_b32 s26, s26, 2\n"
    "v_mfma_f32_16x16x32_bf16 v[68:71], a[156:159], v[172:175], v[68:71]\n"
    "ds_read_b128 v[156:159], v25 offset:47104\n"
    "s_add_u32 s26, s26, s22\n"
    "s_mul_i32 s26, s26, 9216\n"
    "v_mfma_f32_16x16x32_bf16 v[72:75], a[152:155], v[176:179], v[72:75]\n"
    "s_waitcnt vmcnt(13)\n"
    "ds_read_b128 a[160:163], v28 offset:32768\n"
    "ds_read_b128 a[164:167], v28 offset:34816\n"
    "ds_read_b128 a[168:171], v29 offset:32768\n"
    "v_mfma_f32_16x16x32_bf16 v[76:79], a[156:159], v[176:179], v[76:79]\n"
    "s_waitcnt vmcnt(9)\n"
    "s_barrier\n"
    "ds_read_b128 v[160:163], v24 offset:49152\n"
    "v_mfma_f32_16x16x32_bf16 v[80:83], a[152:155], v[180:183], v[80:83]\n"
    "ds_read_b128 a[172:175], v29 offset:34816\n"
    "s_add_u32 s11, s26, 4718592\n"
    "s_add_u32 s26, s25, 2\n"
    "v_mfma_f32_16x16x32_bf16 v[84:87], a[156:159], v[180:183], v[84:87]\n"
    "ds_read_b128 v[164:167], v24 offset:51200\n"
    "s_and_b32 s26, s26, 3\n"
    "s_waitcnt lgkmcnt(9)\n"
    "v_mfma_f32_16x16x32_bf16 v[88:91], a[152:155], v[136:139], v[88:91]\n"
    "buffer_load_dwordx4 v10, s[36:39], s60 offen offset:0 lds\n"
    "v_mfma_f32_16x16x32_bf16 v[92:95], a[156:159], v[136:139], v[92:95]\n"
    "ds_read_b128 v[168:171], v24 offset:53248\n"
    "s_add_u32 s26, s26, s7\n"
    "s_lshl_b32 s26, s26, 2\n"
    "v_mfma_f32_16x16x32_bf16 v[96:99], a[152:155], v[140:143], v[96:99]\n"
    "s_add_u32 s26, s26, s22\n"
    "s_mul_i32 s26, s26, 9216\n"
    "s_add_u32 s12, s26, 4721664\n"
    "v_mfma_f32_16x16x32_bf16 v[100:103], a[156:159], v[140:143], v[100:103]\n"
    "ds_read_b128 v[172:175], v24 offset:55296\n"
    "s_add_u32 s26, s25, 3\n"
    "s_and_b32 s26, s26, 3\n"
    "v_mfma_f32_16x16x32_bf16 v[104:107], a[152:155], v[144:147], v[104:107]\n"
    "buffer_load_dwordx4 v11, s[36:39], s60 offen offset:1024 lds\n"
    "v_mfma_f32_16x16x32_bf16 v[108:111], a[156:159], v[144:147], v[108:111]\n"
    "ds_read_b128 v[176:179], v24 offset:57344\n"
    "s_add_u32 s26, s26, s7\n"
    "s_waitcnt lgkmcnt(9)\n"
    "v_mfma_f32_16x16x32_bf16 v[112:115], a[152:155], v[148:151], v[112:115]\n"
    "s_lshl_b32 s26, s26, 2\n"
    "s_add_u32 s26, s26, s22\n"
    "s_mul_i32 s26, s26, 9216\n"
    "v_mfma_f32_16x16x32_bf16 v[116:119], a[156:159], v[148:151], v[116:119]\n"
    "ds_read_b128 v[180:183], v24 offset:59392\n"
    "s_add_u32 s13, s26, 4724736\n"
    "s_add_u32 s26, s7, s25\n"
    "v_mfma_f32_16x16x32_bf16 v[120:123], a[152:155], v[152:155], v[120:123]\n"
    "buffer_load_dwordx4 v12, s[36:39], s60 offen offset:2048 lds\n"
    "v_mfma_f32_16x16x32_bf16 v[124:127], a[156:159], v[152:155], v[124:127]\n"
    "ds_read_b128 v[136:139], v26 offset:22528\n"
    "s_lshl_b32 s26, s26, 2\n"
    "s_add_u32 s26, s26, s22\n"
    "v_mfma_f32_16x16x32_bf16 v[128:131], a[152:155], v[156:159], v[128:131]\n"
    "s_mul_i32 s26, s26, 9216\n"
    "s_add_u32 s15, s26, 4718592\n"
    "s_sub_u32 s26, s22, s25\n"
    "v_mfma_f32_16x16x32_bf16 v[132:135], a[156:159], v[156:159], v[132:135]\n"
    "ds_read_b128 v[140:143], v24 offset:61440\n"
    "s_add_u32 s26, s26, 3\n"
    "s_waitcnt lgkmcnt(5)\n"
    "v_mfma_f32_16x16x32_bf16 v[64:67], a[160:163], v[160:163], v[64:67]\n"
    "buffer_load_dwordx4 v13, s[36:39], s60 offen offset:3072 lds\n"
    "v_mfma_f32_16x16x32_bf16 v[68:71], a[164:167], v[160:163], v[68:71]\n"
    "ds_read_b128 v[144:147], v24 offset:63488\n"
    "s_add_u32 m0, s59, 16384\n"
    "s_movk_i32 s61, 640\n"
    "v_mfma_f32_16x16x32_bf16 v[72:75], a[160:163], v[164:167], v[72:75]\n"
    "s_and_b32 s26, s26, 3\n"
    "s_mul_i32 s26, s26, 3072\n"
    "s_lshl_b32 s29, s7, 2\n"
    "v_mfma_f32_16x16x32_bf16 v[76:79], a[164:167], v[164:167], v[76:79]\n"
    "ds_read_b128 v[148:151], v25 offset:49152\n"
    "s_mul_i32 s30, s22, 5\n"
    "s_add_u32 s29, s29, s30\n"
    "v_mfma_f32_16x16x32_bf16 v[80:83], a[160:163], v[168:171], v[80:83]\n"
    "buffer_load_dwordx4 v14, s[40:43], s61 offen offset:0 lds\n"
    "v_mfma_f32_16x16x32_bf16 v[84:87], a[164:167], v[168:171], v[84:87]\n"
    "ds_read_b128 v[152:155], v25 offset:51200\n"
    "s_mul_i32 s29, s29, 9216\n"
    "s_waitcnt lgkmcnt(5)\n"
    "v_mfma_f32_16x16x32_bf16 v[88:91], a[160:163], v[172:175], v[88:91]\n"
    "s_add_u32 s26, s26, s29\n"
    "s_add_u32 s26, s26, 4720640\n"
    "v_mfma_f32_16x16x32_bf16 v[92:95], a[164:167], v[172:175], v[92:95]\n"
    "ds_read_b128 v[156:159], v25 offset:53248\n"
    "s_cmp_eq_u32 s22, s25\n"
    "s_cselect_b32 s30, 1, 0\n"
    "v_mfma_f32_16x16x32_bf16 v[96:99], a[160:163], v[176:179], v[96:99]\n"
    "buffer_load_dwordx4 v15, s[40:43], s61 offen offset:1024 lds\n"
    "v_mfma_f32_16x16x32_bf16 v[100:103], a[164:167], v[176:179], v[100:103]\n"
    "ds_read_b128 v[160:163], v25 offset:55296\n"
    "s_and_b32 s30, s30, s22\n"
    "s_mul_i32 s30, s30, 18432\n"
    "v_mfma_f32_16x16x32_bf16 v[104:107], a[160:163], v[180:183], v[104:107]\n"
    "s_sub_u32 s14, s26, s30\n"
    "s_add_u32 s26, s7, s22\n"
    "s_lshl_b32 s26, s26, 11\n"
    "v_mfma_f32_16x16x32_bf16 v[108:111], a[164:167], v[180:183], v[108:111]\n"
    "ds_read_b128 v[164:167], v25 offset:57344\n"
    "s_add_u32 s21, s26, 14680064\n"
    "s_waitcnt lgkmcnt(5)\n"
    "v_mfma_f32_16x16x32_bf16 v[112:115], a[160:163], v[136:139], v[112:115]\n"
    "buffer_load_dwordx4 v16, s[40:43], s61 offen offset:2048 lds\n"
    "v_mfma_f32_16x16x32_bf16 v[116:119], a[164:167], v[136:139], v[116:119]\n"
    "ds_read_b128 v[168:171], v25 offset:59392\n"
    "s_lshl_b32 s29, s24, 2\n"
    "s_add_u32 s29, s29, s25\n"
    "v_mfma_f32_16x16x32_bf16 v[120:123], a[160:163], v[140:143], v[120:123]\n"
    "s_lshl_b32 s29, s29, 6\n"
    "s_add_u32 s16, s18, s29\n"
    "s_add_u32 s17, s16, 18432\n"
    "v_mfma_f32_16x16x32_bf16 v[124:127], a[164:167], v[140:143], v[124:127]\n"
    "ds_read_b128 v[172:175], v27 offset:22528\n"
    "s_lshl_b32 s29, s24, 5\n"
    "s_add_u32 s29, s29, 768\n"
    "v_mfma_f32_16x16x32_bf16 v[128:131], a[160:163], v[144:147], v[128:131]\n"
    "buffer_load_dwordx4 v17, s[40:43], s61 offen offset:3072 lds\n"
    "v_mfma_f32_16x16x32_bf16 v[132:135], a[164:167], v[144:147], v[132:135]\n"
    "ds_read_b128 v[176:179], v25 offset:61440\n"
    "s_add_u32 m0, s59, 98304\n"
    "s_waitcnt lgkmcnt(5)\n"
    "v_mfma_f32_16x16x32_bf16 v[64:67], a[168:171], v[148:151], v[64:67]\n"
    "s_movk_i32 s60, 768\n"
    "s_add_u32 s18, s18, s29\n"
    "v_mfma_f32_16x16x32_bf16 v[68:71], a[172:175], v[148:151], v[68:71]\n"
    "ds_read_b128 v[180:183], v25 offset:63488\n"
    "v_mul_u32_u24 v9, 1152, v2\n"
    "v_mfma_f32_16x16x32_bf16 v[72:75], a[168:171], v[152:155], v[72:75]\n"
    "s_waitcnt vmcnt(13)\n"
    "ds_read_b128 a[176:179], v28 offset:16384\n"
    "ds_read_b128 a[180:183], v28 offset:18432\n"
    "ds_read_b128 a[184:187], v29 offset:16384\n"
    "v_mfma_f32_16x16x32_bf16 v[76:79], a[172:175], v[152:155], v[76:79]\n"
    "s_waitcnt vmcnt(8)\n"
    "s_barrier\n"
    "ds_read_b128 v[136:139], v24 offset:0\n"
    "v_mfma_f32_16x16x32_bf16 v[80:83], a[168:171], v[156:159], v[80:83]\n"
    "ds_read_b128 a[188:191], v29 offset:18432\n"
    "v_lshl_add_u32 v46, v4, 4, v9\n"
    "v_mfma_f32_16x16x32_bf16 v[84:87], a[172:175], v[156:159], v[84:87]\n"
    "ds_read_b128 v[140:143], v24 offset:2048\n"
    "s_waitcnt lgkmcnt(9)\n"
    "v_mfma_f32_16x16x32_bf16 v[88:91], a[168:171], v[160:163], v[88:91]\n"
    "buffer_load_dwordx4 v10, s[36:39], s60 offen offset:0 lds\n"
    "v_mfma_f32_16x16x32_bf16 v[92:95], a[172:175], v[160:163], v[92:95]\n"
    "ds_read_b128 v[144:147], v24 offset:4096\n"
    "v_lshl_add_u32 v47, v4, 3, v9\n"
    "v_mfma_f32_16x16x32_bf16 v[96:99], a[168:171], v[164:167], v[96:99]\n"
    "v_and_b32 v9, 1, v2\n"
    "v_lshlrev_b32 v9, 4, v9\n"
    "v_mfma_f32_16x16x32_bf16 v[100:103], a[172:175], v[164:167], v[100:103]\n"
    "ds_read_b128 v[148:151], v24 offset:6144\n"
    "v_mov_b32 v8, 0x3f80\n"
    "v_mfma_f32_16x16x32_bf16 v[104:107], a[168:171], v[168:171], v[104:107]\n"
    "buffer_load_dwordx4 v11, s[36:39], s60 offen offset:1024 lds\n"
    "v_mfma_f32_16x16x32_bf16 v[108:111], a[172:175], v[168:171], v[108:111]\n"
    "ds_read_b128 v[152:155], v24 offset:8192\n"
    "s_waitcnt lgkmcnt(9)\n"
    "v_mfma_f32_16x16x32_bf16 v[112:115], a[168:171], v[172:175], v[112:115]\n"
    "v_lshlrev_b32 v8, v9, v8\n"
    "v_lshrrev_b32 v9, 2, v2\n"
    "v_mfma_f32_16x16x32_bf16 v[116:119], a[172:175], v[172:175], v[116:119]\n"
    "ds_read_b128 v[156:159], v24 offset:10240\n"
    "v_mfma_f32_16x16x32_bf16 v[120:123], a[168:171], v[176:179], v[120:123]\n"
    "buffer_load_dwordx4 v12, s[36:39], s60 offen offset:2048 lds\n"
    "v_mfma_f32_16x16x32_bf16 v[124:127], a[172:175], v[176:179], v[124:127]\n"
    "ds_read_b128 v[160:163], v26 offset:24576\n"
    "v_mfma_f32_16x16x32_bf16 v[128:131], a[168:171], v[180:183], v[128:131]\n"
    "v_cmp_eq_u32 vcc, v9, v4\n"
    "s_nop 1\n"
    "v_cndmask_b32 v8, 0, v8, vcc\n"
    "v_mfma_f32_16x16x32_bf16 v[132:135], a[172:175], v[180:183], v[132:135]\n"
    "ds_read_b128 v[164:167], v24 offset:12288\n"
    "s_waitcnt lgkmcnt(5)\n"
    "v_mfma_f32_16x16x32_bf16 v[64:67], a[176:179], v[136:139], v[64:67]\n"
    "buffer_load_dwordx4 v13, s[36:39], s60 offen offset:3072 lds\n"
    "v_mfma_f32_16x16x32_bf16 v[68:71], a[180:183], v[136:139], v[68:71]\n"
    "ds_read_b128 v[168:171], v24 offset:14336\n"
    "s_add_u32 m0, s59, 32768\n"
    "s_movk_i32 s61, 768\n"
    "v_mfma_f32_16x16x32_bf16 v[72:75], a[176:179], v[140:143], v[72:75]\n"
    "v_and_b32 v9, 2, v2\n"
    "v_mfma_f32_16x16x32_bf16 v[76:79], a[180:183], v[140:143], v[76:79]\n"
    "ds_read_b128 v[172:175], v25 offset:0\n"
    "v_mfma_f32_16x16x32_bf16 v[80:83], a[176:179], v[144:147], v[80:83]\n"
    "buffer_load_dwordx4 v14, s[40:43], s61 offen offset:0 lds\n"
    "v_mfma_f32_16x16x32_bf16 v[84:87], a[180:183], v[144:147], v[84:87]\n"
    "ds_read_b128 v[176:179], v25 offset:2048\n"
    "s_waitcnt lgkmcnt(5)\n"
    "v_mfma_f32_16x16x32_bf16 v[88:91], a[176:179], v[148:151], v[88:91]\n"
    "v_cmp_eq_u32 vcc, 0, v9\n"
    "v_mov_b32 v50, 0\n"
    "v_mov_b32 v51, 0\n"
    "v_cndmask_b32 v48, 0, v8, vcc\n"
    "v_mfma_f32_16x16x32_bf16 v[92:95], a[180:183], v[148:151], v[92:95]\n"
    "ds_read_b128 v[180:183], v25 offset:4096\n"
    "v_sub_u32 v49, v8, v48\n"
    "v_mfma_f32_16x16x32_bf16 v[96:99], a[176:179], v[152:155], v[96:99]\n"
    "buffer_load_dwordx4 v15, s[40:43], s61 offen offset:1024 lds\n"
    "v_mfma_f32_16x16x32_bf16 v[100:103], a[180:183], v[152:155], v[100:103]\n"
    "ds_read_b128 v[136:139], v25 offset:6144\n"
    "v_mov_b32 v52, 0\n"
    "v_mfma_f32_16x16x32_bf16 v[104:107], a[176:179], v[156:159], v[104:107]\n"
    "v_mov_b32 v53, 0\n"
    "v_mov_b32 v54, v48\n"
    "v_mfma_f32_16x16x32_bf16 v[108:111], a[180:183], v[156:159], v[108:111]\n"
    "ds_read_b128 v[140:143], v25 offset:8192\n"
    "s_waitcnt lgkmcnt(5)\n"
    "v_mfma_f32_16x16x32_bf16 v[112:115], a[176:179], v[160:163], v[112:115]\n"
    "buffer_load_dwordx4 v16, s[40:43], s61 offen offset:2048 lds\n"
    "v_mfma_f32_16x16x32_bf16 v[116:119], a[180:183], v[160:163], v[116:119]\n"
    "ds_read_b128 v[144:147], v25 offset:10240\n"
    "v_mov_b32 v55, v49\n"
    "v_mfma_f32_16x16x32_bf16 v[120:123], a[176:179], v[164:167], v[120:123]\n"
    "buffer_load_dwordx4 v17, s[40:43], s61 offen offset:3072 lds\n"
    "v_mfma_f32_16x16x32_bf16 v[124:127], a[180:183], v[164:167], v[124:127]\n"
    "ds_read_b128 v[148:151], v27 offset:24576\n"
    "s_add_u32 m0, s33, 147456\n"
    "s_movk_i32 s61, 768\n"
    "v_mfma_f32_16x16x32_bf16 v[128:131], a[176:179], v[168:171], v[128:131]\n"
    "buffer_load_dwordx4 v18, s[40:43], s61 offen offset:0 lds\n"
    "v_mfma_f32_16x16x32_bf16 v[132:135], a[180:183], v[168:171], v[132:135]\n"
    "ds_read_b128 v[152:155], v25 offset:12288\n"
    "s_add_u32 m0, s59, 81920\n"
    "s_waitcnt lgkmcnt(5)\n"
    "v_mfma_f32_16x16x32_bf16 v[64:67], a[184:187], v[172:175], v[64:67]\n"
    "s_movk_i32 s60, 896\n"
    "s_add_u32 s26, s25, 1\n"
    "s_xor_b32 s26, s26, s25\n"
    "v_mfma_f32_16x16x32_bf16 v[68:71], a[188:191], v[172:175], v[68:71]\n"
    "ds_read_b128 v[156:159], v25 offset:14336\n"
    "s_bitcmp1_b32 s26, 1\n"
    "s_cselect_b32 s26, 0x80008000, 0\n"
    "v_mfma_f32_16x16x32_bf16 v[72:75], a[184:187], v[176:179], v[72:75]\n"
    "s_waitcnt vmcnt(13)\n"
    "ds_read_b128 a[192:195], v28 offset:0\n"
    "ds_read_b128 a[196:199], v28 offset:2048\n"
    "ds_read_b128 a[200:203], v29 offset:0\n"
    "v_mfma_f32_16x16x32_bf16 v[76:79], a[188:191], v[176:179], v[76:79]\n"
    "s_waitcnt vmcnt(9)\n"
    "s_barrier\n"
    "ds_read_b128 v[160:163], v24 offset:16384\n"
    "v_mfma_f32_16x16x32_bf16 v[80:83], a[184:187], v[180:183], v[80:83]\n"
    "ds_read_b128 a[204:207], v29 offset:2048\n"
    "v_xor_b32 v192, s26, v48\n"
    "v_mfma_f32_16x16x32_bf16 v[84:87], a[188:191], v[180:183], v[84:87]\n"
    "ds_read_b128 v[164:167], v24 offset:18432\n"
    "s_waitcnt lgkmcnt(9)\n"
    "v_mfma_f32_16x16x32_bf16 v[88:91], a[184:187], v[136:139], v[88:91]\n"
    "buffer_load_dwordx4 v10, s[36:39], s60 offen offset:0 lds\n"
    "v_mfma_f32_16x16x32_bf16 v[92:95], a[188:191], v[136:139], v[92:95]\n"
    "ds_read_b128 v[168:171], v24 offset:20480\n"
    "v_xor_b32 v193, s26, v49\n"
    "v_mfma_f32_16x16x32_bf16 v[96:99], a[184:187], v[140:143], v[96:99]\n"
    "v_mov_b32 v194, 0\n"
    "v_mov_b32 v195, 0\n"
    "v_mfma_f32_16x16x32_bf16 v[100:103], a[188:191], v[140:143], v[100:103]\n"
    "ds_read_b128 v[172:175], v24 offset:22528\n"
    "v_mov_b32 v196, 0\n"
    "v_mfma_f32_16x16x32_bf16 v[104:107], a[184:187], v[144:147], v[104:107]\n"
    "buffer_load_dwordx4 v11, s[36:39], s60 offen offset:1024 lds\n"
    "v_mfma_f32_16x16x32_bf16 v[108:111], a[188:191], v[144:147], v[108:111]\n"
    "ds_read_b128 v[176:179], v24 offset:24576\n"
    "s_waitcnt lgkmcnt(9)\n"
    "v_mfma_f32_16x16x32_bf16 v[112:115], a[184:187], v[148:151], v[112:115]\n"
    "v_mov_b32 v197, 0\n"
    "v_xor_b32 v198, s26, v54\n"
    "v_mfma_f32_16x16x32_bf16 v[116:119], a[188:191], v[148:151], v[116:119]\n"
    "ds_read_b128 v[180:183], v24 offset:26624\n"
    "v_xor_b32 v199, s26, v55\n"
    "v_mfma_f32_16x16x32_bf16 v[120:123], a[184:187], v[152:155], v[120:123]\n"
    "buffer_load_dwordx4 v12, s[36:39], s60 offen offset:2048 lds\n"
    "v_mfma_f32_16x16x32_bf16 v[124:127], a[188:191], v[152:155], v[124:127]\n"
    "ds_read_b128 v[136:139], v26 offset:26624\n"
    "s_add_u32 s26, s25, 2\n"
    "s_xor_b32 s26, s26, s25\n"
    "v_mfma_f32_16x16x32_bf16 v[128:131], a[184:187], v[156:159], v[128:131]\n"
    "s_bitcmp1_b32 s26, 1\n"
    "s_cselect_b32 s26, 0x80008000, 0\n"
    "v_mfma_f32_16x16x32_bf16 v[132:135], a[188:191], v[156:159], v[132:135]\n"
    "ds_read_b128 v[140:143], v24 offset:28672\n"
    "s_waitcnt lgkmcnt(5)\n"
    "v_mfma_f32_16x16x32_bf16 v[64:67], a[192:195], v[160:163], v[64:67]\n"
    "buffer_load_dwordx4 v13, s[36:39], s60 offen offset:3072 lds\n"
    "v_mfma_f32_16x16x32_bf16 v[68:71], a[196:199], v[160:163], v[68:71]\n"
    "ds_read_b128 v[144:147], v24 offset:30720\n"
    "s_add_u32 m0, s59, 49152\n"
    "s_movk_i32 s61, 896\n"
    "v_mfma_f32_16x16x32_bf16 v[72:75], a[192:195], v[164:167], v[72:75]\n"
    "v_xor_b32 v184, s26, v48\n"
    "v_xor_b32 v185, s26, v49\n"
    "v_mfma_f32_16x16x32_bf16 v[76:79], a[196:199], v[164:167], v[76:79]\n"
    "ds_read_b128 v[148:151], v25 offset:16384\n"
    "v_mov_b32 v186, 0\n"
    "v_mfma_f32_16x16x32_bf16 v[80:83], a[192:195], v[168:171], v[80:83]\n"
    "buffer_load_dwordx4 v14, s[40:43], s61 offen offset:0 lds\n"
    "v_mfma_f32_16x16x32_bf16 v[84:87], a[196:199], v[168:171], v[84:87]\n"
    "ds_read_b128 v[152:155], v25 offset:18432\n"
    "s_waitcnt lgkmcnt(5)\n"
    "v_mfma_f32_16x16x32_bf16 v[88:91], a[192:195], v[172:175], v[88:91]\n"
    "v_mov_b32 v187, 0\n"
    "v_mov_b32 v188, 0\n"
    "v_mfma_f32_16x16x32_bf16 v[92:95], a[196:199], v[172:175], v[92:95]\n"
    "ds_read_b128 v[156:159], v25 offset:20480\n"
    "v_mov_b32 v189, 0\n"
    "v_mfma_f32_16x16x32_bf16 v[96:99], a[192:195], v[176:179], v[96:99]\n"
    "buffer_load_dwordx4 v15, s[40:43], s61 offen offset:1024 lds\n"
    "v_mfma_f32_16x16x32_bf16 v[100:103], a[196:199], v[176:179], v[100:103]\n"
    "ds_read_b128 v[160:163], v25 offset:22528\n"
    "v_xor_b32 v190, s26, v54\n"
    "v_mfma_f32_16x16x32_bf16 v[104:107], a[192:195], v[180:183], v[104:107]\n"
    "v_xor_b32 v191, s26, v55\n"
    "s_add_u32 s26, s25, 3\n"
    "v_mfma_f32_16x16x32_bf16 v[108:111], a[196:199], v[180:183], v[108:111]\n"
    "ds_read_b128 v[164:167], v25 offset:24576\n"
    "s_xor_b32 s26, s26, s25\n"
    "s_waitcnt lgkmcnt(5)\n"
    "v_mfma_f32_16x16x32_bf16 v[112:115], a[192:195], v[136:139], v[112:115]\n"
    "buffer_load_dwordx4 v16, s[40:43], s61 offen offset:2048 lds\n"
    "v_mfma_f32_16x16x32_bf16 v[116:119], a[196:199], v[136:139], v[116:119]\n"
    "ds_read_b128 v[168:171], v25 offset:26624\n"
    "s_bitcmp1_b32 s26, 1\n"
    "s_cselect_b32 s26, 0x80008000, 0\n"
    "v_mfma_f32_16x16x32_bf16 v[120:123], a[192:195], v[140:143], v[120:123]\n"
    "v_xor_b32 v48, s26, v48\n"
    "v_xor_b32 v49, s26, v49\n"
    "v_mfma_f32_16x16x32_bf16 v[124:127], a[196:199], v[140:143], v[124:127]\n"
    "ds_read_b128 v[172:175], v27 offset:26624\n"
    "v_xor_b32 v54, s26, v54\n"
    "v_mfma_f32_16x16x32_bf16 v[128:131], a[192:195], v[144:147], v[128:131]\n"
    "buffer_load_dwordx4 v17, s[40:43], s61 offen offset:3072 lds\n"
    "v_mfma_f32_16x16x32_bf16 v[132:135], a[196:199], v[144:147], v[132:135]\n"
    "ds_read_b128 v[176:179], v25 offset:28672\n"
    "s_add_u32 m0, s59, 65536\n"
    "s_waitcnt lgkmcnt(5)\n"
    "v_mfma_f32_16x16x32_bf16 v[64:67], a[200:203], v[148:151], v[64:67]\n"
    "s_movk_i32 s60, 1024\n"
    "v_xor_b32 v55, s26, v55\n"
    "v_mfma_f32_16x16x32_bf16 v[68:71], a[204:207], v[148:151], v[68:71]\n"
    "ds_read_b128 v[180:183], v25 offset:30720\n"
    "s_add_u32 s26, s15, 3072\n"
    "s_add_u32 s27, s15, 6144\n"
    "v_mfma_f32_16x16x32_bf16 v[72:75], a[200:203], v[152:155], v[72:75]\n"
    "s_waitcnt vmcnt(13)\n"
    "ds_read_b128 a[208:211], v28 offset:32768\n"
    "ds_read_b128 a[212:215], v28 offset:34816\n"
    "ds_read_b128 a[216:219], v29 offset:32768\n"
    "v_mfma_f32_16x16x32_bf16 v[76:79], a[204:207], v[152:155], v[76:79]\n"
    "s_waitcnt vmcnt(8)\n"
    "s_barrier\n"
    "ds_read_b128 v[136:139], v24 offset:32768\n"
    "v_mfma_f32_16x16x32_bf16 v[80:83], a[200:203], v[156:159], v[80:83]\n"
    "ds_read_b128 a[220:223], v29 offset:34816\n"
    "s_lshl_b32 s29, s25, 8\n"
    "s_add_u32 s29, s29, s21\n"
    "v_mfma_f32_16x16x32_bf16 v[84:87], a[204:207], v[156:159], v[84:87]\n"
    "ds_read_b128 v[140:143], v24 offset:34816\n"
    "s_waitcnt lgkmcnt(9)\n"
    "v_mfma_f32_16x16x32_bf16 v[88:91], a[200:203], v[160:163], v[88:91]\n"
    "buffer_load_dwordx4 v10, s[36:39], s60 offen offset:0 lds\n"
    "v_mfma_f32_16x16x32_bf16 v[92:95], a[204:207], v[160:163], v[92:95]\n"
    "ds_read_b128 v[144:147], v24 offset:36864\n"
    "s_cmp_eq_u32 s22, s25\n"
    "s_cselect_b64 s[30:31], -1, 1\n"
    "v_mfma_f32_16x16x32_bf16 v[96:99], a[200:203], v[164:167], v[96:99]\n"
    "v_mfma_f32_16x16x32_bf16 v[100:103], a[204:207], v[164:167], v[100:103]\n"
    "ds_read_b128 v[148:151], v24 offset:38912\n"
    "v_mfma_f32_16x16x32_bf16 v[104:107], a[200:203], v[168:171], v[104:107]\n"
    "buffer_load_dwordx4 v11, s[36:39], s60 offen offset:1024 lds\n"
    "v_mfma_f32_16x16x32_bf16 v[108:111], a[204:207], v[168:171], v[108:111]\n"
    "ds_read_b128 v[152:155], v24 offset:40960\n"
    "s_waitcnt lgkmcnt(9)\n"
    "v_mfma_f32_16x16x32_bf16 v[112:115], a[200:203], v[172:175], v[112:115]\n"
    "v_mfma_f32_16x16x32_bf16 v[116:119], a[204:207], v[172:175], v[116:119]\n"
    "ds_read_b128 v[156:159], v24 offset:43008\n"
    "v_mfma_f32_16x16x32_bf16 v[120:123], a[200:203], v[176:179], v[120:123]\n"
    "buffer_load_dwordx4 v12, s[36:39], s60 offen offset:2048 lds\n"
    "v_mfma_f32_16x16x32_bf16 v[124:127], a[204:207], v[176:179], v[124:127]\n"
    "ds_read_b128 v[160:163], v26 offset:16384\n"
    "v_mfma_f32_16x16x32_bf16 v[128:131], a[200:203], v[180:183], v[128:131]\n"
    "v_mfma_f32_16x16x32_bf16 v[132:135], a[204:207], v[180:183], v[132:135]\n"
    "ds_read_b128 v[164:167], v24 offset:45056\n"
    "s_waitcnt lgkmcnt(5)\n"
    "v_mfma_f32_16x16x32_bf16 v[64:67], a[208:211], v[136:139], v[64:67]\n"
    "buffer_load_dwordx4 v13, s[36:39], s60 offen offset:3072 lds\n"
    "v_mfma_f32_16x16x32_bf16 v[68:71], a[212:215], v[136:139], v[68:71]\n"
    "ds_read_b128 v[168:171], v24 offset:47104\n"
    "s_add_u32 m0, s59, 0\n"
    "s_movk_i32 s61, 1024\n"
    "v_mfma_f32_16x16x32_bf16 v[72:75], a[208:211], v[140:143], v[72:75]\n"
    "v_mfma_f32_16x16x32_bf16 v[76:79], a[212:215], v[140:143], v[76:79]\n"
    "ds_read_b128 v[172:175], v25 offset:32768\n"
    "v_mfma_f32_16x16x32_bf16 v[80:83], a[208:211], v[144:147], v[80:83]\n"
    "buffer_load_dwordx4 v14, s[40:43], s61 offen offset:0 lds\n"
    "v_mfma_f32_16x16x32_bf16 v[84:87], a[212:215], v[144:147], v[84:87]\n"
    "ds_read_b128 v[176:179], v25 offset:34816\n"
    "s_waitcnt lgkmcnt(5)\n"
    "v_mfma_f32_16x16x32_bf16 v[88:91], a[208:211], v[148:151], v[88:91]\n"
    "v_mfma_f32_16x16x32_bf16 v[92:95], a[212:215], v[148:151], v[92:95]\n"
    "ds_read_b128 v[180:183], v25 offset:36864\n"
    "v_mfma_f32_16x16x32_bf16 v[96:99], a[208:211], v[152:155], v[96:99]\n"
    "buffer_load_dwordx4 v15, s[40:43], s61 offen offset:1024 lds\n"
    "v_mfma_f32_16x16x32_bf16 v[100:103], a[212:215], v[152:155], v[100:103]\n"
    "ds_read_b128 v[136:139], v25 offset:38912\n"
    "v_mfma_f32_16x16x32_bf16 v[104:107], a[208:211], v[156:159], v[104:107]\n"
    "v_mfma_f32_16x16x32_bf16 v[108:111], a[212:215], v[156:159], v[108:111]\n"
    "ds_read_b128 v[140:143], v25 offset:40960\n"
    "s_waitcnt lgkmcnt(5)\n"
    "v_mfma_f32_16x16x32_bf16 v[112:115], a[208:211], v[160:163], v[112:115]\n"
    "buffer_load_dwordx4 v16, s[40:43], s61 offen offset:2048 lds\n"
    "v_mfma_f32_16x16x32_bf16 v[116:119], a[212:215], v[160:163], v[116:119]\n"
    "ds_read_b128 v[144:147], v25 offset:43008\n"
    "v_mfma_f32_16x16x32_bf16 v[120:123], a[208:211], v[164:167], v[120:123]\n"
    "buffer_load_dwordx4 v17, s[40:43], s61 offen offset:3072 lds\n"
    "v_mfma_f32_16x16x32_bf16 v[124:127], a[212:215], v[164:167], v[124:127]\n"
    "ds_read_b128 v[148:151], v27 offset:16384\n"
    "s_add_u32 m0, s33, 151552\n"
    "s_movk_i32 s61, 1024\n"
    "v_mfma_f32_16x16x32_bf16 v[128:131], a[208:211], v[168:171], v[128:131]\n"
    "buffer_load_dwordx4 v36, s[40:43], s61 offen offset:0 lds\n"
    "v_mfma_f32_16x16x32_bf16 v[132:135], a[212:215], v[168:171], v[132:135]\n"
    "ds_read_b128 v[152:155], v25 offset:45056\n"
    "s_waitcnt lgkmcnt(5)\n"
    "v_mfma_f32_16x16x32_bf16 v[64:67], a[216:219], v[172:175], v[64:67]\n"
    "v_mfma_f32_16x16x32_bf16 v[68:71], a[220:223], v[172:175], v[68:71]\n"
    "ds_read_b128 v[156:159], v25 offset:47104\n"
    "v_mfma_f32_16x16x32_bf16 v[72:75], a[216:219], v[176:179], v[72:75]\n"
    "s_waitcnt vmcnt(13)\n"
    "ds_read_b128 a[224:227], v28 offset:16384\n"
    "ds_read_b128 a[228:231], v28 offset:18432\n"
    "ds_read_b128 a[232:235], v29 offset:16384\n"
    "v_mfma_f32_16x16x32_bf16 v[76:79], a[220:223], v[176:179], v[76:79]\n"
    "s_waitcnt vmcnt(9)\n"
    "s_barrier\n"
    "ds_read_b128 v[160:163], v24 offset:49152\n"
    "v_mfma_f32_16x16x32_bf16 v[80:83], a[216:219], v[180:183], v[80:83]\n"
    "ds_read_b128 a[236:239], v29 offset:18432\n"
    "v_mfma_f32_16x16x32_bf16 v[84:87], a[220:223], v[180:183], v[84:87]\n"
    "ds_read_b128 v[164:167], v24 offset:51200\n"
    "s_waitcnt lgkmcnt(9)\n"
    "v_mfma_f32_16x16x32_bf16 v[88:91], a[216:219], v[136:139], v[88:91]\n"
    "v_mfma_f32_16x16x32_bf16 v[92:95], a[220:223], v[136:139], v[92:95]\n"
    "ds_read_b128 v[168:171], v24 offset:53248\n"
    "v_mfma_f32_16x16x32_bf16 v[96:99], a[216:219], v[140:143], v[96:99]\n"
    "v_mfma_f32_16x16x32_bf16 v[100:103], a[220:223], v[140:143], v[100:103]\n"
    "ds_read_b128 v[172:175], v24 offset:55296\n"
    "v_mfma_f32_16x16x32_bf16 v[104:107], a[216:219], v[144:147], v[104:107]\n"
    "v_mfma_f32_16x16x32_bf16 v[108:111], a[220:223], v[144:147], v[108:111]\n"
    "ds_read_b128 v[176:179], v24 offset:57344\n"
    "s_waitcnt lgkmcnt(9)\n"
    "v_mfma_f32_16x16x32_bf16 v[112:115], a[216:219], v[148:151], v[112:115]\n"
    "v_mfma_f32_16x16x32_bf16 v[116:119], a[220:223], v[148:151], v[116:119]\n"
    "ds_read_b128 v[180:183], v24 offset:59392\n"
    "v_mfma_f32_16x16x32_bf16 v[120:123], a[216:219], v[152:155], v[120:123]\n"
    "v_mfma_f32_16x16x32_bf16 v[124:127], a[220:223], v[152:155], v[124:127]\n"
    "ds_read_b128 v[136:139], v26 offset:18432\n"
    "v_mfma_f32_16x16x32_bf16 v[128:131], a[216:219], v[156:159], v[128:131]\n"
    "v_mfma_f32_16x16x32_bf16 v[132:135], a[220:223], v[156:159], v[132:135]\n"
    "ds_read_b128 v[140:143], v24 offset:61440\n"
    "s_waitcnt lgkmcnt(5)\n"
    "v_mfma_f32_16x16x32_bf16 v[64:67], a[224:227], v[160:163], v[64:67]\n"
    "v_mfma_f32_16x16x32_bf16 v[68:71], a[228:231], v[160:163], v[68:71]\n"
    "ds_read_b128 v[144:147], v24 offset:63488\n"
    "v_mfma_f32_16x16x32_bf16 v[72:75], a[224:227], v[164:167], v[72:75]\n"
    "v_mfma_f32_16x16x32_bf16 v[76:79], a[228:231], v[164:167], v[76:79]\n"
    "ds_read_b128 v[148:151], v25 offset:49152\n"
    "v_mfma_f32_16x16x32_bf16 v[80:83], a[224:227], v[168:171], v[80:83]\n"
    "v_mfma_f32_16x16x32_bf16 v[84:87], a[228:231], v[168:171], v[84:87]\n"
    "ds_read_b128 v[152:155], v25 offset:51200\n"
    "s_waitcnt lgkmcnt(5)\n"
    "v_mfma_f32_16x16x32_bf16 v[88:91], a[224:227], v[172:175], v[88:91]\n"
    "v_mfma_f32_16x16x32_bf16 v[92:95], a[228:231], v[172:175], v[92:95]\n"
    "ds_read_b128 v[156:159], v25 offset:53248\n"
    "v_mfma_f32_16x16x32_bf16 v[96:99], a[224:227], v[176:179], v[96:99]\n"
    "v_mfma_f32_16x16x32_bf16 v[100:103], a[228:231], v[176:179], v[100:103]\n"
    "ds_read_b128 v[160:163], v25 offset:55296\n"
    "v_mfma_f32_16x16x32_bf16 v[104:107], a[224:227], v[180:183], v[104:107]\n"
    "v_mfma_f32_16x16x32_bf16 v[108:111], a[228:231], v[180:183], v[108:111]\n"
    "ds_read_b128 v[164:167], v25 offset:57344\n"
    "s_waitcnt lgkmcnt(5)\n"
    "v_mfma_f32_16x16x32_bf16 v[112:115], a[224:227], v[136:139], v[112:115]\n"
    "v_mfma_f32_16x16x32_bf16 v[116:119], a[228:231], v[136:139], v[116:119]\n"
    "ds_read_b128 v[168:171], v25 offset:59392\n"
    "v_mfma_f32_16x16x32_bf16 v[120:123], a[224:227], v[140:143], v[120:123]\n"
    "v_mfma_f32_16x16x32_bf16 v[124:127], a[228:231], v[140:143], v[124:127]\n"
    "ds_read_b128 v[172:175], v27 offset:18432\n"
    "v_mfma_f32_16x16x32_bf16 v[128:131], a[224:227], v[144:147], v[128:131]\n"
    "v_mfma_f32_16x16x32_bf16 v[132:135], a[228:231], v[144:147], v[132:135]\n"
    "ds_read_b128 v[176:179], v25 offset:61440\n"
    "s_waitcnt lgkmcnt(5)\n"
    "v_mfma_f32_16x16x32_bf16 v[64:67], a[232:235], v[148:151], v[64:67]\n"
    "v_mfma_f32_16x16x32_bf16 v[68:71], a[236:239], v[148:151], v[68:71]\n"
    "ds_read_b128 v[180:183], v25 offset:63488\n"
    "v_mfma_f32_16x16x32_bf16 v[72:75], a[232:235], v[152:155], v[72:75]\n"
    "s_waitcnt vmcnt(5)\n"
    "ds_read_b128 a[240:243], v28 offset:0\n"
    "ds_read_b128 a[244:247], v28 offset:2048\n"
    "ds_read_b128 a[248:251], v29 offset:0\n"
    "v_mfma_f32_16x16x32_bf16 v[76:79], a[236:239], v[152:155], v[76:79]\n"
    "ds_read_b128 a[252:255], v29 offset:2048\n"
    "v_mfma_f32_16x16x32_bf16 v[80:83], a[232:235], v[156:159], v[80:83]\n"
    "v_mfma_f32_16x16x32_bf16 v[84:87], a[236:239], v[156:159], v[84:87]\n"
    "s_waitcnt lgkmcnt(7)\n"
    "v_mfma_f32_16x16x32_bf16 v[88:91], a[232:235], v[160:163], v[88:91]\n"
    "v_mfma_f32_16x16x32_bf16 v[92:95], a[236:239], v[160:163], v[92:95]\n"
    "v_mfma_f32_16x16x32_bf16 v[96:99], a[232:235], v[164:167], v[96:99]\n"
    "v_mfma_f32_16x16x32_bf16 v[100:103], a[236:239], v[164:167], v[100:103]\n"
    "v_mfma_f32_16x16x32_bf16 v[104:107], a[232:235], v[168:171], v[104:107]\n"
    "v_mfma_f32_16x16x32_bf16 v[108:111], a[236:239], v[168:171], v[108:111]\n"
    "s_waitcnt vmcnt(0)\n"
    "s_barrier\n"
    "ds_read_b128 v[136:139], v24 offset:0\n"
    "s_waitcnt lgkmcnt(5)\n"
    "v_mfma_f32_16x16x32_bf16 v[112:115], a[232:235], v[172:175], v[112:115]\n"
    "v_mfma_f32_16x16x32_bf16 v[116:119], a[236:239], v[172:175], v[116:119]\n"
    "ds_read_b128 v[140:143], v25 offset:0\n"
    "v_mfma_f32_16x16x32_bf16 v[120:123], a[232:235], v[176:179], v[120:123]\n"
    "v_mfma_f32_16x16x32_bf16 v[124:127], a[236:239], v[176:179], v[124:127]\n"
    "ds_read_b128 v[144:147], v24 offset:2048\n"
    "v_mfma_f32_16x16x32_bf16 v[128:131], a[232:235], v[180:183], v[128:131]\n"
    "v_mfma_f32_16x16x32_bf16 v[132:135], a[236:239], v[180:183], v[132:135]\n"
    "ds_read_b128 v[148:151], v25 offset:2048\n"
    "s_waitcnt lgkmcnt(1)\n"
    "v_mfma_f32_16x16x32_bf16 v[64:67], a[240:243], v[136:139], v[64:67]\n"
    "v_mfma_f32_16x16x32_bf16 v[68:71], a[244:247], v[136:139], v[68:71]\n"
    "ds_read_b128 v[152:155], v24 offset:4096\n"
    "v_mfma_f32_16x16x32_bf16 v[64:67], a[248:251], v[140:143], v[64:67]\n"
    "v_mfma_f32_16x16x32_bf16 v[68:71], a[252:255], v[140:143], v[68:71]\n"
    "ds_read_b128 v[156:159], v25 offset:4096\n"
    "v_mfma_f32_16x16x32_bf16 v[72:75], a[240:243], v[144:147], v[72:75]\n"
    "v_mfma_f32_16x16x32_bf16 v[76:79], a[244:247], v[144:147], v[76:79]\n"
    "ds_read_b128 v[160:163], v24 offset:6144\n"
    "s_waitcnt lgkmcnt(1)\n"
    "v_mfma_f32_16x16x32_bf16 v[72:75], a[248:251], v[148:151], v[72:75]\n"
    "v_mfma_f32_16x16x32_bf16 v[76:79], a[252:255], v[148:151], v[76:79]\n"
    "ds_read_b128 v[164:167], v25 offset:6144\n"
    "v_mfma_f32_16x16x32_bf16 v[80:83], a[240:243], v[152:155], v[80:83]\n"
    "v_cvt_pk_bf16_f32 v200, v64, v65\n"
    "v_cvt_pk_bf16_f32 v201, v66, v67\n"
    "v_cvt_pk_bf16_f32 v202, v68, v69\n"
    "v_mfma_f32_16x16x32_bf16 v[84:87], a[244:247], v[152:155], v[84:87]\n"
    "ds_read_b128 v[168:171], v24 offset:8192\n"
    "v_cvt_pk_bf16_f32 v203, v70, v71\n"
    "v_mfma_f32_16x16x32_bf16 v[80:83], a[248:251], v[156:159], v[80:83]\n"
    "buffer_store_dwordx4 v[200:203], v23, s[48:51], s8 offen offset:0 sc1\n"
    "v_mfma_f32_16x16x32_bf16 v[84:87], a[252:255], v[156:159], v[84:87]\n"
    "ds_read_b128 v[172:175], v25 offset:8192\n"
    "s_waitcnt lgkmcnt(1)\n"
    "v_mfma_f32_16x16x32_bf16 v[88:91], a[240:243], v[160:163], v[88:91]\n"
    "v_cvt_pk_bf16_f32 v204, v72, v73\n"
    "v_cvt_pk_bf16_f32 v205, v74, v75\n"
    "v_cvt_pk_bf16_f32 v206, v76, v77\n"
    "v_mfma_f32_16x16x32_bf16 v[92:95], a[244:247], v[160:163], v[92:95]\n"
    "ds_read_b128 v[176:179], v24 offset:10240\n"
    "v_cvt_pk_bf16_f32 v207, v78, v79\n"
    "v_mfma_f32_16x16x32_bf16 v[88:91], a[248:251], v[164:167], v[88:91]\n"
    "buffer_store_dwordx4 v[204:207], v23, s[48:51], s8 offen offset:1024 sc1\n"
    "v_mfma_f32_16x16x32_bf16 v[92:95], a[252:255], v[164:167], v[92:95]\n"
    "ds_read_b128 v[180:183], v25 offset:10240\n"
    "v_mfma_f32_16x16x32_bf16 v[96:99], a[240:243], v[168:171], v[96:99]\n"
    "v_cvt_pk_bf16_f32 v208, v80, v81\n"
    "v_cvt_pk_bf16_f32 v209, v82, v83\n"
    "v_cvt_pk_bf16_f32 v210, v84, v85\n"
    "v_mfma_f32_16x16x32_bf16 v[100:103], a[244:247], v[168:171], v[100:103]\n"
    "ds_read_b128 v[136:139], v26 offset:20480\n"
    "v_cvt_pk_bf16_f32 v211, v86, v87\n"
    "s_waitcnt lgkmcnt(1)\n"
    "v_mfma_f32_16x16x32_bf16 v[96:99], a[248:251], v[172:175], v[96:99]\n"
    "buffer_store_dwordx4 v[208:211], v23, s[48:51], s8 offen offset:2048 sc1\n"
    "v_mfma_f32_16x16x32_bf16 v[100:103], a[252:255], v[172:175], v[100:103]\n"
    "ds_read_b128 v[140:143], v27 offset:20480\n"
    "v_mfma_f32_16x16x32_bf16 v[104:107], a[240:243], v[176:179], v[104:107]\n"
    "v_cvt_pk_bf16_f32 v212, v88, v89\n"
    "v_cvt_pk_bf16_f32 v213, v90, v91\n"
    "v_cvt_pk_bf16_f32 v214, v92, v93\n"
    "v_mfma_f32_16x16x32_bf16 v[108:111], a[244:247], v[176:179], v[108:111]\n"
    "ds_read_b128 v[144:147], v24 offset:12288\n"
    "v_cvt_pk_bf16_f32 v215, v94, v95\n"
    "v_mfma_f32_16x16x32_bf16 v[104:107], a[248:251], v[180:183], v[104:107]\n"
    "buffer_store_dwordx4 v[212:215], v23, s[48:51], s8 offen offset:3072 sc1\n"
    "v_mfma_f32_16x16x32_bf16 v[108:111], a[252:255], v[180:183], v[108:111]\n"
    "ds_read_b128 v[148:151], v25 offset:12288\n"
    "s_waitcnt lgkmcnt(1)\n"
    "v_mfma_f32_16x16x32_bf16 v[112:115], a[240:243], v[136:139], v[112:115]\n"
    "v_cvt_pk_bf16_f32 v216, v96, v97\n"
    "v_cvt_pk_bf16_f32 v217, v98, v99\n"
    "v_cvt_pk_bf16_f32 v218, v100, v101\n"
    "v_mfma_f32_16x16x32_bf16 v[116:119], a[244:247], v[136:139], v[116:119]\n"
    "ds_read_b128 v[152:155], v24 offset:14336\n"
    "v_cvt_pk_bf16_f32 v219, v102, v103\n"
    "v_mfma_f32_16x16x32_bf16 v[112:115], a[248:251], v[140:143], v[112:115]\n"
    "buffer_store_dwordx4 v[216:219], v23, s[48:51], s9 offen offset:0 sc1\n"
    "v_mfma_f32_16x16x32_bf16 v[116:119], a[252:255], v[140:143], v[116:119]\n"
    "ds_read_b128 v[156:159], v25 offset:14336\n"
    "v_mfma_f32_16x16x32_bf16 v[120:123], a[240:243], v[144:147], v[120:123]\n"
    "v_cvt_pk_bf16_f32 v220, v104, v105\n"
    "v_cvt_pk_bf16_f32 v221, v106, v107\n"
    "v_cvt_pk_bf16_f32 v222, v108, v109\n"
    "v_mfma_f32_16x16x32_bf16 v[124:127], a[244:247], v[144:147], v[124:127]\n"
    "v_cvt_pk_bf16_f32 v223, v110, v111\n"
    "s_waitcnt lgkmcnt(0)\n"
    "v_mfma_f32_16x16x32_bf16 v[120:123], a[248:251], v[148:151], v[120:123]\n"
    "buffer_store_dwordx4 v[220:223], v23, s[48:51], s9 offen offset:1024 sc1\n"
    "v_mfma_f32_16x16x32_bf16 v[124:127], a[252:255], v[148:151], v[124:127]\n"
    "v_mfma_f32_16x16x32_bf16 v[128:131], a[240:243], v[152:155], v[128:131]\n"
    "v_cvt_pk_bf16_f32 v224, v112, v113\n"
    "v_cvt_pk_bf16_f32 v225, v114, v115\n"
    "v_cvt_pk_bf16_f32 v226, v116, v117\n"
    "v_mfma_f32_16x16x32_bf16 v[132:135], a[244:247], v[152:155], v[132:135]\n"
    "v_cvt_pk_bf16_f32 v227, v118, v119\n"
    "v_mfma_f32_16x16x32_bf16 v[128:131], a[248:251], v[156:159], v[128:131]\n"
    "buffer_store_dwordx4 v[224:227], v23, s[48:51], s9 offen offset:2048 sc1\n"
    "v_mfma_f32_16x16x32_bf16 v[132:135], a[252:255], v[156:159], v[132:135]\n"
    "v_cvt_pk_bf16_f32 v228, v120, v121\n"
    "v_cvt_pk_bf16_f32 v229, v122, v123\n"
    "v_cvt_pk_bf16_f32 v230, v124, v125\n"
    "v_cvt_pk_bf16_f32 v231, v126, v127\n"
    "buffer_store_dwordx4 v[228:231], v23, s[48:51], s10 offen offset:0 sc1\n"
    "s_nop 7\n"
    "v_cvt_pk_bf16_f32 v232, v128, v129\n"
    "v_cvt_pk_bf16_f32 v233, v130, v131\n"
    "v_cvt_pk_bf16_f32 v234, v132, v133\n"
    "v_cvt_pk_bf16_f32 v235, v134, v135\n"
    "buffer_store_dwordx4 v[232:235], v23, s[48:51], s10 offen offset:1024 sc1\n"
    "buffer_load_dwordx2 v[38:39], v45, s[48:51], s19 offen sc1\n"
    "s_add_u32 m0, s59, 65536\n"
    "s_add_u32 s63, s62, 0\n"
    "s_and_b32 s63, s63, 2047\n"
    "buffer_load_dwordx4 v19, s[44:47], s63 offen offset:0 lds\n"
    "buffer_load_dwordx4 v20, s[44:47], s63 offen offset:1024 lds\n"
    "buffer_load_dwordx4 v21, s[44:47], s63 offen offset:2048 lds\n"
    "buffer_load_dwordx4 v22, s[44:47], s63 offen offset:3072 lds\n"
    "s_add_u32 m0, s59, 81920\n"
    "s_add_u32 s63, s62, 128\n"
    "s_and_b32 s63, s63, 2047\n"
    "buffer_load_dwordx4 v19, s[44:47], s63 offen offset:0 lds\n"
    "buffer_load_dwordx4 v20, s[44:47], s63 offen offset:1024 lds\n"
    "buffer_load_dwordx4 v21, s[44:47], s63 offen offset:2048 lds\n"
    "buffer_load_dwordx4 v22, s[44:47], s63 offen offset:3072 lds\n"
    "ds_write_b128 v34, v[200:203] offset:0\n"
    "ds_write_b128 v34, v[204:207] offset:1024\n"
    "ds_write_b128 v34, v[208:211] offset:2048\n"
    "ds_write_b128 v34, v[212:215] offset:3072\n"
    "ds_write_b128 v33, v[216:219] offset:16384\n"
    "ds_write_b128 v33, v[220:223] offset:17408\n"
    "ds_write_b128 v33, v[224:227] offset:18432\n"
    "s_waitcnt vmcnt(8)\n"
    "v_add_co_u32 v38, vcc, 1, v38\n"
    "s_nop 1\n"
    "v_addc_co_u32 v39, vcc, 0, v39, vcc\n"
    "s_barrier\n"
    "s_cmp_eq_u32 s22, 0\n"
    "s_cbranch_scc0 L5_%=\n"
    "s_mov_b64 exec, 1\n"
    "buffer_store_dwordx2 v[38:39], v45, s[48:51], s20 offen sc1\n"
    "s_mov_b64 exec, -1\n"
    "L5_%=:\n"
    "s_add_u32 m0, s59, 98304\n"
    "s_add_u32 s63, s62, 256\n"
    "s_and_b32 s63, s63, 2047\n"
    "buffer_load_dwordx4 v19, s[44:47], s63 offen offset:0 lds\n"
    "buffer_load_dwordx4 v20, s[44:47], s63 offen offset:1024 lds\n"
    "buffer_load_dwordx4 v21, s[44:47], s63 offen offset:2048 lds\n"
    "buffer_load_dwordx4 v22, s[44:47], s63 offen offset:3072 lds\n"
    "s_waitcnt vmcnt(8)\n"
    "ds_read_b128 a[0:3], v28 offset:0\n"
    "ds_read_b128 a[4:7], v28 offset:2048\n"
    "ds_read_b128 a[8:11], v29 offset:0\n"
    "ds_read_b128 a[12:15], v29 offset:2048\n"
    "s_add_u32 m0, s59, 114688\n"
    "s_add_u32 s63, s62, 384\n"
    "s_and_b32 s63, s63, 2047\n"
    "buffer_load_dwordx4 v19, s[44:47], s63 offen offset:0 lds\n"
    "buffer_load_dwordx4 v20, s[44:47], s63 offen offset:1024 lds\n"
    "buffer_load_dwordx4 v21, s[44:47], s63 offen offset:2048 lds\n"
    "buffer_load_dwordx4 v22, s[44:47], s63 offen offset:3072 lds\n"
    "s_waitcnt vmcnt(8)\n"
    "ds_read_b128 a[16:19], v28 offset:16384\n"
    "ds_read_b128 a[20:23], v28 offset:18432\n"
    "ds_read_b128 a[24:27], v29 offset:16384\n"
    "ds_read_b128 a[28:31], v29 offset:18432\n"
    "s_waitcnt lgkmcnt(0)\n"
    "s_add_u32 m0, s59, 65536\n"
    "s_add_u32 s63, s62, 512\n"
    "s_and_b32 s63, s63, 2047\n"
    "buffer_load_dwordx4 v19, s[44:47], s63 offen offset:0 lds\n"
    "buffer_load_dwordx4 v20, s[44:47], s63 offen offset:1024 lds\n"
    "buffer_load_dwordx4 v21, s[44:47], s63 offen offset:2048 lds\n"
    "buffer_load_dwordx4 v22, s[44:47], s63 offen offset:3072 lds\n"
    "s_waitcnt vmcnt(8)\n"
    "ds_read_b128 a[32:35], v28 offset:32768\n"
    "ds_read_b128 a[36:39], v28 offset:34816\n"
    "ds_read_b128 a[40:43], v29 offset:32768\n"
    "ds_read_b128 a[44:47], v29 offset:34816\n"
    "s_add_u32 m0, s59, 81920\n"
    "s_add_u32 s63, s62, 640\n"
    "s_and_b32 s63, s63, 2047\n"
    "buffer_load_dwordx4 v19, s[44:47], s63 offen offset:0 lds\n"
    "buffer_load_dwordx4 v20, s[44:47], s63 offen offset:1024 lds\n"
    "buffer_load_dwordx4 v21, s[44:47], s63 offen offset:2048 lds\n"
    "buffer_load_dwordx4 v22, s[44:47], s63 offen offset:3072 lds\n"
    "s_waitcnt vmcnt(8)\n"
    "ds_read_b128 a[48:51], v28 offset:49152\n"
    "ds_read_b128 a[52:55], v28 offset:51200\n"
    "ds_read_b128 a[56:59], v29 offset:49152\n"
    "ds_read_b128 a[60:63], v29 offset:51200\n"
    "buffer_load_dwordx2 v[40:41], v42, s[48:51], 0 offen offset:0 sc1\n"
    "s_waitcnt lgkmcnt(0)\n"
    "s_add_u32 m0, s59, 98304\n"
    "s_add_u32 s63, s62, 768\n"
    "s_and_b32 s63, s63, 2047\n"
    "buffer_load_dwordx4 v19, s[44:47], s63 offen offset:0 lds\n"
    "buffer_load_dwordx4 v20, s[44:47], s63 offen offset:1024 lds\n"
    "buffer_load_dwordx4 v21, s[44:47], s63 offen offset:2048 lds\n"
    "buffer_load_dwordx4 v22, s[44:47], s63 offen offset:3072 lds\n"
    "s_waitcnt vmcnt(9)\n"
    "ds_read_b128 a[64:67], v28 offset:0\n"
    "ds_read_b128 a[68:71], v28 offset:2048\n"
    "ds_read_b128 a[72:75], v29 offset:0\n"
    "ds_read_b128 a[76:79], v29 offset:2048\n"
    "s_add_u32 m0, s59, 114688\n"
    "s_add_u32 s63, s62, 896\n"
    "s_and_b32 s63, s63, 2047\n"
    "buffer_load_dwordx4 v19, s[44:47], s63 offen offset:0 lds\n"
    "buffer_load_dwordx4 v20, s[44:47], s63 offen offset:1024 lds\n"
    "buffer_load_dwordx4 v21, s[44:47], s63 offen offset:2048 lds\n"
    "buffer_load_dwordx4 v22, s[44:47], s63 offen offset:3072 lds\n"
    "s_waitcnt vmcnt(9)\n"
    "ds_read_b128 a[80:83], v28 offset:16384\n"
    "ds_read_b128 a[84:87], v28 offset:18432\n"
    "ds_read_b128 a[88:91], v29 offset:16384\n"
    "ds_read_b128 a[92:95], v29 offset:18432\n"
    "s_waitcnt vmcnt(8)\n"
    "s_cmp_eq_u32 s22, 0\n"
    "s_cbranch_scc0 L6_%=\n"
    "v_cmp_ne_u64 vcc, v[40:41], v[38:39]\n"
    "s_cbranch_vccnz L7_%=\n"
    "L6_%=:\n"
    "s_barrier\n"
    "s_add_u32 m0, s59, 147456\n"
    "s_add_u32 s67, s69, 4\n"
    "s_and_b32 s67, s67, 31\n"
    "s_mul_i32 s66, s67, 4096\n"
    "s_add_u32 s66, s66, s68\n"
    "buffer_load_dwordx4 v23, s[48:51], s66 offen offset:0 sc1 lds\n"
    "buffer_load_dwordx4 v23, s[48:51], s66 offen offset:1024 sc1 lds\n"
    "buffer_load_dwordx4 v23, s[48:51], s66 offen offset:2048 sc1 lds\n"
    "buffer_load_dwordx4 v23, s[48:51], s66 offen offset:3072 sc1 lds\n"
    "s_add_u32 m0, s59, 0\n"
    "s_add_u32 s67, s69, 4\n"
    "s_and_b32 s67, s67, 31\n"
    "s_mul_i32 s66, s67, 3072\n"
    "s_add_u32 s66, s66, s68\n"
    "s_add_u32 s66, s66, 131072\n"
    "buffer_load_dwordx4 v23, s[48:51], s66 offen offset:0 sc1 lds\n"
    "buffer_load_dwordx4 v23, s[48:51], s66 offen offset:1024 sc1 lds\n"
    "buffer_load_dwordx4 v23, s[48:51], s66 offen offset:2048 sc1 lds\n"
    "s_barrier\n"
    "ds_read_b128 v[136:139], v32 offset:0\n"
    "ds_read_b128 v[140:143], v32 offset:1024\n"
    "ds_read_b128 v[144:147], v32 offset:2048\n"
    "ds_read_b128 v[148:151], v32 offset:3072\n"
    "ds_read_b128 v[152:155], v32 offset:4096\n"
    "ds_read_b128 v[156:159], v32 offset:5120\n"
    "ds_read_b128 v[160:163], v32 offset:6144\n"
    "ds_read_b128 v[164:167], v32 offset:7168\n"
    "s_waitcnt lgkmcnt(5)\n"
    "v_mfma_f32_16x16x32_bf16 v[64:67], v[136:139], a[0:3], 0\n"
    "s_add_u32 m0, s59, 65536\n"
    "s_add_u32 s63, s62, 1024\n"
    "s_and_b32 s63, s63, 2047\n"
    "v_mfma_f32_16x16x32_bf16 v[68:71], v[136:139], a[4:7], 0\n"
    "ds_read_b128 v[168:171], v32 offset:8192\n"
    "v_mfma_f32_16x16x32_bf16 v[72:75], v[140:143], a[0:3], 0\n"
    "buffer_load_dwordx4 v19, s[44:47], s63 offen offset:0 lds\n"
    "v_mfma_f32_16x16x32_bf16 v[76:79], v[140:143], a[4:7], 0\n"
    "ds_read_b128 v[172:175], v32 offset:9216\n"
    "v_mfma_f32_16x16x32_bf16 v[80:83], v[144:147], a[0:3], 0\n"
    "v_mfma_f32_16x16x32_bf16 v[84:87], v[144:147], a[4:7], 0\n"
    "ds_read_b128 v[176:179], v32 offset:10240\n"
    "s_waitcnt lgkmcnt(5)\n"
    "v_mfma_f32_16x16x32_bf16 v[88:91], v[148:151], a[0:3], 0\n"
    "buffer_load_dwordx4 v20, s[44:47], s63 offen offset:1024 lds\n"
    "v_mfma_f32_16x16x32_bf16 v[92:95], v[148:151], a[4:7], 0\n"
    "ds_read_b128 v[180:183], v32 offset:11264\n"
    "v_mfma_f32_16x16x32_bf16 v[64:67], v[152:155], a[8:11], v[64:67]\n"
    "v_mfma_f32_16x16x32_bf16 v[68:71], v[152:155], a[12:15], v[68:71]\n"
    "ds_read_b128 v[136:139], v32 offset:12288\n"
    "v_mfma_f32_16x16x32_bf16 v[72:75], v[156:159], a[8:11], v[72:75]\n"
    "s_waitcnt vmcnt(13)\n"
    "ds_read_b128 a[96:99], v28 offset:32768\n"
    "ds_read_b128 a[100:103], v28 offset:34816\n"
    "ds_read_b128 a[104:107], v29 offset:32768\n"
    "v_mfma_f32_16x16x32_bf16 v[76:79], v[156:159], a[12:15], v[76:79]\n"
    "ds_read_b128 v[140:143], v32 offset:13312\n"
    "ds_read_b128 a[108:111], v29 offset:34816\n"
    "s_waitcnt lgkmcnt(9)\n"
    "v_mfma_f32_16x16x32_bf16 v[80:83], v[160:163], a[8:11], v[80:83]\n"
    "buffer_load_dwordx4 v21, s[44:47], s63 offen offset:2048 lds\n"
    "v_mfma_f32_16x16x32_bf16 v[84:87], v[160:163], a[12:15], v[84:87]\n"
    "ds_read_b128 v[144:147], v32 offset:14336\n"
    "v_mfma_f32_16x16x32_bf16 v[88:91], v[164:167], a[8:11], v[88:91]\n"
    "v_mfma_f32_16x16x32_bf16 v[92:95], v[164:167], a[12:15], v[92:95]\n"
    "ds_read_b128 v[148:151], v32 offset:15360\n"
    "v_mfma_f32_16x16x32_bf16 v[64:67], v[168:171], a[16:19], v[64:67]\n"
    "buffer_load_dwordx4 v22, s[44:47], s63 offen offset:3072 lds\n"
    "v_mfma_f32_16x16x32_bf16 v[68:71], v[168:171], a[20:23], v[68:71]\n"
    "ds_read_b128 v[152:155], v30 offset:16384\n"
    "s_add_u32 m0, s59, 81920\n"
    "s_waitcnt lgkmcnt(9)\n"
    "v_mfma_f32_16x16x32_bf16 v[72:75], v[172:175], a[16:19], v[72:75]\n"
    "s_add_u32 s63, s62, 1152\n"
    "s_and_b32 s63, s63, 2047\n"
    "v_mfma_f32_16x16x32_bf16 v[76:79], v[172:175], a[20:23], v[76:79]\n"
    "ds_read_b128 v[156:159], v30 offset:17408\n"
    "v_mfma_f32_16x16x32_bf16 v[80:83], v[176:179], a[16:19], v[80:83]\n"
    "buffer_load_dwordx4 v19, s[44:47], s63 offen offset:0 lds\n"
    "v_mfma_f32_16x16x32_bf16 v[84:87], v[176:179], a[20:23], v[84:87]\n"
    "ds_read_b128 v[160:163], v30 offset:18432\n"
    "v_mfma_f32_16x16x32_bf16 v[88:91], v[180:183], a[16:19], v[88:91]\n"
    "v_mfma_f32_16x16x32_bf16 v[92:95], v[180:183], a[20:23], v[92:95]\n"
    "ds_read_b128 v[164:167], v30 offset:20480\n"
    "s_waitcnt lgkmcnt(5)\n"
    "v_mfma_f32_16x16x32_bf16 v[64:67], v[136:139], a[24:27], v[64:67]\n"
    "buffer_load_dwordx4 v20, s[44:47], s63 offen offset:1024 lds\n"
    "v_mfma_f32_16x16x32_bf16 v[68:71], v[136:139], a[28:31], v[68:71]\n"
    "ds_read_b128 v[168:171], v30 offset:21504\n"
    "v_mfma_f32_16x16x32_bf16 v[72:75], v[140:143], a[24:27], v[72:75]\n"
    "v_mfma_f32_16x16x32_bf16 v[76:79], v[140:143], a[28:31], v[76:79]\n"
    "ds_read_b128 v[172:175], v30 offset:22528\n"
    "v_mfma_f32_16x16x32_bf16 v[80:83], v[144:147], a[24:27], v[80:83]\n"
    "buffer_load_dwordx4 v21, s[44:47], s63 offen offset:2048 lds\n"
    "v_mfma_f32_16x16x32_bf16 v[84:87], v[144:147], a[28:31], v[84:87]\n"
    "ds_read_b128 v[176:179], v30 offset:24576\n"
    "s_waitcnt lgkmcnt(5)\n"
    "v_mfma_f32_16x16x32_bf16 v[88:91], v[148:151], a[24:27], v[88:91]\n"
    "v_mfma_f32_16x16x32_bf16 v[92:95], v[148:151], a[28:31], v[92:95]\n"
    "ds_read_b128 v[180:183], v30 offset:25600\n"
    "v_mfma_f32_16x16x32_bf16 v[96:99], v[152:155], a[0:3], 0\n"
    "buffer_load_dwordx4 v22, s[44:47], s63 offen offset:3072 lds\n"
    "v_mfma_f32_16x16x32_bf16 v[100:103], v[152:155], a[4:7], 0\n"
    "ds_read_b128 v[136:139], v30 offset:26624\n"
    "s_add_u32 m0, s59, 32768\n"
    "s_add_u32 s67, s69, 8\n"
    "v_mfma_f32_16x16x32_bf16 v[104:107], v[156:159], a[0:3], 0\n"
    "s_and_b32 s67, s67, 31\n"
    "s_mul_i32 s66, s67, 4096\n"
    "s_add_u32 s66, s66, s68\n"
    "v_mfma_f32_16x16x32_bf16 v[108:111], v[156:159], a[4:7], 0\n"
    "ds_read_b128 v[140:143], v30 offset:28672\n"
    "s_waitcnt lgkmcnt(5)\n"
    "v_mfma_f32_16x16x32_bf16 v[112:115], v[160:163], a[0:3], 0\n"
    "s_waitcnt vmcnt(15)\n"
    "ds_read_b128 a[112:115], v28 offset:49152\n"
    "ds_read_b128 a[116:119], v28 offset:51200\n"
    "ds_read_b128 a[120:123], v29 offset:49152\n"
    "v_mfma_f32_16x16x32_bf16 v[116:119], v[160:163], a[4:7], 0\n"
    "ds_read_b128 v[144:147], v30 offset:29696\n"
    "ds_read_b128 a[124:127], v29 offset:51200\n"
    "v_mfma_f32_16x16x32_bf16 v[96:99], v[164:167], a[8:11], v[96:99]\n"
    "buffer_load_dwordx4 v23, s[48:51], s66 offen offset:0 sc1 lds\n"
    "v_mfma_f32_16x16x32_bf16 v[100:103], v[164:167], a[12:15], v[100:103]\n"
    "ds_read_b128 v[148:151], v30 offset:30720\n"
    "v_mfma_f32_16x16x32_bf16 v[104:107], v[168:171], a[8:11], v[104:107]\n"
    "v_mfma_f32_16x16x32_bf16 v[108:111], v[168:171], a[12:15], v[108:111]\n"
    "s_waitcnt vmcnt(9)\n"
    "s_barrier\n"
    "ds_read_b128 v[152:155], v32 offset:16384\n"
    "s_waitcnt lgkmcnt(9)\n"
    "v_mfma_f32_16x16x32_bf16 v[112:115], v[172:175], a[8:11], v[112:115]\n"
    "buffer_load_dwordx4 v23, s[48:51], s66 offen offset:1024 sc1 lds\n"
    "v_mfma_f32_16x16x32_bf16 v[116:119], v[172:175], a[12:15], v[116:119]\n"
    "ds_read_b128 v[156:159], v32 offset:17408\n"
    "v_mfma_f32_16x16x32_bf16 v[96:99], v[176:179], a[16:19], v[96:99]\n"
    "v_mfma_f32_16x16x32_bf16 v[100:103], v[176:179], a[20:23], v[100:103]\n"
    "ds_read_b128 v[160:163], v32 offset:18432\n"
    "v_mfma_f32_16x16x32_bf16 v[104:107], v[180:183], a[16:19], v[104:107]\n"
    "buffer_load_dwordx4 v23, s[48:51], s66 offen offset:2048 sc1 lds\n"
    "v_mfma_f32_16x16x32_bf16 v[108:111], v[180:183], a[20:23], v[108:111]\n"
    "ds_read_b128 v[164:167], v32 offset:19456\n"
    "s_waitcnt lgkmcnt(6)\n"
    "v_mfma_f32_16x16x32_bf16 v[112:115], v[136:139], a[16:19], v[112:115]\n"
    "v_mfma_f32_16x16x32_bf16 v[116:119], v[136:139], a[20:23], v[116:119]\n"
    "ds_read_b128 v[168:171], v32 offset:20480\n"
    "v_mfma_f32_16x16x32_bf16 v[96:99], v[140:143], a[24:27], v[96:99]\n"
    "buffer_load_dwordx4 v23, s[48:51], s66 offen offset:3072 sc1 lds\n"
    "v_mfma_f32_16x16x32_bf16 v[100:103], v[140:143], a[28:31], v[100:103]\n"
    "ds_read_b128 v[172:175], v32 offset:21504\n"
    "s_add_u32 m0, s59, 131072\n"
    "s_add_u32 s67, s69, 8\n"
    "v_mfma_f32_16x16x32_bf16 v[104:107], v[144:147], a[24:27], v[104:107]\n"
    "s_and_b32 s67, s67, 31\n"
    "s_mul_i32 s66, s67, 3072\n"
    "s_add_u32 s66, s66, s68\n"
    "v_mfma_f32_16x16x32_bf16 v[108:111], v[144:147], a[28:31], v[108:111]\n"
    "ds_read_b128 v[176:179], v32 offset:22528\n"
    "s_add_u32 s66, s66, 131072\n"
    "s_waitcnt lgkmcnt(5)\n"
    "v_mfma_f32_16x16x32_bf16 v[112:115], v[148:151], a[24:27], v[112:115]\n"
    "buffer_load_dwordx4 v23, s[48:51], s66 offen offset:0 sc1 lds\n"
    "v_mfma_f32_16x16x32_bf16 v[116:119], v[148:151], a[28:31], v[116:119]\n"
    "ds_read_b128 v[180:183], v32 offset:23552\n"
    "v_mfma_f32_16x16x32_bf16 v[64:67], v[152:155], a[32:35], v[64:67]\n"
    "buffer_load_dwordx4 v23, s[48:51], s66 offen offset:1024 sc1 lds\n"
    "v_mfma_f32_16x16x32_bf16 v[68:71], v[152:155], a[36:39], v[68:71]\n"
    "ds_read_b128 v[136:139], v32 offset:24576\n"
    "v_mfma_f32_16x16x32_bf16 v[72:75], v[156:159], a[32:35], v[72:75]\n"
    "buffer_load_dwordx4 v23, s[48:51], s66 offen offset:2048 sc1 lds\n"
    "v_mfma_f32_16x16x32_bf16 v[76:79], v[156:159], a[36:39], v[76:79]\n"
    "ds_read_b128 v[140:143], v32 offset:25600\n"
    "s_add_u32 m0, s59, 98304\n"
    "s_waitcnt lgkmcnt(5)\n"
    "v_mfma_f32_16x16x32_bf16 v[80:83], v[160:163], a[32:35], v[80:83]\n"
    "s_add_u32 s63, s62, 1280\n"
    "s_and_b32 s63, s63, 2047\n"
    "v_mfma_f32_16x16x32_bf16 v[84:87], v[160:163], a[36:39], v[84:87]\n"
    "ds_read_b128 v[144:147], v32 offset:26624\n"
    "v_mfma_f32_16x16x32_bf16 v[88:91], v[164:167], a[32:35], v[88:91]\n"
    "s_waitcnt vmcnt(11)\n"
    "ds_read_b128 a[128:131], v28 offset:0\n"
    "ds_read_b128 a[132:135], v28 offset:2048\n"
    "ds_read_b128 a[136:139], v29 offset:0\n"
    "v_mfma_f32_16x16x32_bf16 v[92:95], v[164:167], a[36:39], v[92:95]\n"
    "ds_read_b128 v[148:151], v32 offset:27648\n"
    "ds_read_b128 a[140:143], v29 offset:2048\n"
    "v_mfma_f32_16x16x32_bf16 v[64:67], v[168:171], a[40:43], v[64:67]\n"
    "buffer_load_dwordx4 v19, s[44:47], s63 offen offset:0 lds\n"
    "v_mfma_f32_16x16x32_bf16 v[68:71], v[168:171], a[44:47], v[68:71]\n"
    "ds_read_b128 v[152:155], v32 offset:28672\n"
    "s_waitcnt lgkmcnt(9)\n"
    "v_mfma_f32_16x16x32_bf16 v[72:75], v[172:175], a[40:43], v[72:75]\n"
    "v_mfma_f32_16x16x32_bf16 v[76:79], v[172:175], a[44:47], v[76:79]\n"
    "ds_read_b128 v[156:159], v32 offset:29696\n"
    "v_mfma_f32_16x16x32_bf16 v[80:83], v[176:179], a[40:43], v[80:83]\n"
    "buffer_load_dwordx4 v20, s[44:47], s63 offen offset:1024 lds\n"
    "v_mfma_f32_16x16x32_bf16 v[84:87], v[176:179], a[44:47], v[84:87]\n"
    "ds_read_b128 v[160:163], v32 offset:30720\n"
    "v_mfma_f32_16x16x32_bf16 v[88:91], v[180:183], a[40:43], v[88:91]\n"
    "v_mfma_f32_16x16x32_bf16 v[92:95], v[180:183], a[44:47], v[92:95]\n"
    "ds_read_b128 v[164:167], v32 offset:31744\n"
    "s_waitcnt lgkmcnt(9)\n"
    "v_mfma_f32_16x16x32_bf16 v[64:67], v[136:139], a[48:51], v[64:67]\n"
    "buffer_load_dwordx4 v21, s[44:47], s63 offen offset:2048 lds\n"
    "v_mfma_f32_16x16x32_bf16 v[68:71], v[136:139], a[52:55], v[68:71]\n"
    "ds_read_b128 v[168:171], v30 offset:0\n"
    "v_mfma_f32_16x16x32_bf16 v[72:75], v[140:143], a[48:51], v[72:75]\n"
    "buffer_load_dwordx4 v22, s[44:47], s63 offen offset:3072 lds\n"
    "v_mfma_f32_16x16x32_bf16 v[76:79], v[140:143], a[52:55], v[76:79]\n"
    "ds_read_b128 v[172:175], v30 offset:1024\n"
    "s_add_u32 m0, s59, 114688\n"
    "s_add_u32 s63, s62, 1408\n"
    "v_mfma_f32_16x16x32_bf16 v[80:83], v[144:147], a[48:51], v[80:83]\n"
    "s_and_b32 s63, s63, 2047\n"
    "v_mfma_f32_16x16x32_bf16 v[84:87], v[144:147], a[52:55], v[84:87]\n"
    "ds_read_b128 v[176:179], v30 offset:2048\n"
    "s_waitcnt lgkmcnt(5)\n"
    "v_mfma_f32_16x16x32_bf16 v[88:91], v[148:151], a[48:51], v[88:91]\n"
    "buffer_load_dwordx4 v19, s[44:47], s63 offen offset:0 lds\n"
    "v_mfma_f32_16x16x32_bf16 v[92:95], v[148:151], a[52:55], v[92:95]\n"
    "ds_read_b128 v[180:183], v30 offset:4096\n"
    "v_mfma_f32_16x16x32_bf16 v[64:67], v[152:155], a[56:59], v[64:67]\n"
    "v_mfma_f32_16x16x32_bf16 v[68:71], v[152:155], a[60:63], v[68:71]\n"
    "ds_read_b128 v[136:139], v30 offset:5120\n"
    "v_mfma_f32_16x16x32_bf16 v[72:75], v[156:159], a[56:59], v[72:75]\n"
    "buffer_load_dwordx4 v20, s[44:47], s63 offen offset:1024 lds\n"
    "v_mfma_f32_16x16x32_bf16 v[76:79], v[156:159], a[60:63], v[76:79]\n"
    "ds_read_b128 v[140:143], v30 offset:6144\n"
    "s_waitcnt lgkmcnt(5)\n"
    "v_mfma_f32_16x16x32_bf16 v[80:83], v[160:163], a[56:59], v[80:83]\n"
    "v_mfma_f32_16x16x32_bf16 v[84:87], v[160:163], a[60:63], v[84:87]\n"
    "ds_read_b128 v[144:147], v30 offset:8192\n"
    "v_mfma_f32_16x16x32_bf16 v[88:91], v[164:167], a[56:59], v[88:91]\n"
    "buffer_load_dwordx4 v21, s[44:47], s63 offen offset:2048 lds\n"
    "v_mfma_f32_16x16x32_bf16 v[92:95], v[164:167], a[60:63], v[92:95]\n"
    "ds_read_b128 v[148:151], v30 offset:9216\n"
    "v_mfma_f32_16x16x32_bf16 v[96:99], v[168:171], a[32:35], v[96:99]\n"
    "s_waitcnt vmcnt(14)\n"
    "ds_read_b128 a[144:147], v28 offset:16384\n"
    "ds_read_b128 a[148:151], v28 offset:18432\n"
    "ds_read_b128 a[152:155], v29 offset:16384\n"
    "v_mfma_f32_16x16x32_bf16 v[100:103], v[168:171], a[36:39], v[100:103]\n"
    "ds_read_b128 v[152:155], v30 offset:10240\n"
    "ds_read_b128 a[156:159], v29 offset:18432\n"
    "s_waitcnt lgkmcnt(9)\n"
    "v_mfma_f32_16x16x32_bf16 v[104:107], v[172:175], a[32:35], v[104:107]\n"
    "buffer_load_dwordx4 v22, s[44:47], s63 offen offset:3072 lds\n"
    "v_mfma_f32_16x16x32_bf16 v[108:111], v[172:175], a[36:39], v[108:111]\n"
    "ds_read_b128 v[156:159], v30 offset:12288\n"
    "s_add_u32 m0, s59, 49152\n"
    "s_add_u32 s67, s69, 12\n"
    "v_mfma_f32_16x16x32_bf16 v[112:115], v[176:179], a[32:35], v[112:115]\n"
    "s_and_b32 s67, s67, 31\n"
    "s_mul_i32 s66, s67, 4096\n"
    "s_add_u32 s66, s66, s68\n"
    "v_mfma_f32_16x16x32_bf16 v[116:119], v[176:179], a[36:39], v[116:119]\n"
    "ds_read_b128 v[160:163], v30 offset:13312\n"
    "v_mfma_f32_16x16x32_bf16 v[96:99], v[180:183], a[40:43], v[96:99]\n"
    "buffer_load_dwordx4 v23, s[48:51], s66 offen offset:0 sc1 lds\n"
    "v_mfma_f32_16x16x32_bf16 v[100:103], v[180:183], a[44:47], v[100:103]\n"
    "ds_read_b128 v[164:167], v30 offset:14336\n"
    "s_waitcnt lgkmcnt(9)\n"
    "v_mfma_f32_16x16x32_bf16 v[104:107], v[136:139], a[40:43], v[104:107]\n"
    "v_mfma_f32_16x16x32_bf16 v[108:111], v[136:139], a[44:47], v[108:111]\n"
    "s_waitcnt vmcnt(9)\n"
    "s_barrier\n"
    "ds_read_b128 v[168:171], v30 offset:32768\n"
    "v_mfma_f32_16x16x32_bf16 v[112:115], v[140:143], a[40:43], v[112:115]\n"
    "buffer_load_dwordx4 v23, s[48:51], s66 offen offset:1024 sc1 lds\n"
    "v_mfma_f32_16x16x32_bf16 v[116:119], v[140:143], a[44:47], v[116:119]\n"
    "ds_read_b128 v[172:175], v30 offset:33792\n"
    "v_mfma_f32_16x16x32_bf16 v[96:99], v[144:147], a[48:51], v[96:99]\n"
    "v_mfma_f32_16x16x32_bf16 v[100:103], v[144:147], a[52:55], v[100:103]\n"
    "ds_read_b128 v[176:179], v30 offset:34816\n"
    "s_waitcnt lgkmcnt(5)\n"
    "v_mfma_f32_16x16x32_bf16 v[104:107], v[148:151], a[48:51], v[104:107]\n"
    "buffer_load_dwordx4 v23, s[48:51], s66 offen offset:2048 sc1 lds\n"
    "v_mfma_f32_16x16x32_bf16 v[108:111], v[148:151], a[52:55], v[108:111]\n"
    "ds_read_b128 v[180:183], v30 offset:35840\n"
    "v_mfma_f32_16x16x32_bf16 v[112:115], v[152:155], a[48:51], v[112:115]\n"
    "buffer_load_dwordx4 v23, s[48:51], s66 offen offset:3072 sc1 lds\n"
    "v_mfma_f32_16x16x32_bf16 v[116:119], v[152:155], a[52:55], v[116:119]\n"
    "ds_read_b128 v[136:139], v30 offset:36864\n"
    "s_add_u32 m0, s59, 147456\n"
    "s_add_u32 s67, s69, 16\n"
    "v_mfma_f32_16x16x32_bf16 v[96:99], v[156:159], a[56:59], v[96:99]\n"
    "s_and_b32 s67, s67, 31\n"
    "s_mul_i32 s66, s67, 4096\n"
    "s_add_u32 s66, s66, s68\n"
    "v_mfma_f32_16x16x32_bf16 v[100:103], v[156:159], a[60:63], v[100:103]\n"
    "ds_read_b128 v[140:143], v30 offset:37888\n"
    "s_waitcnt lgkmcnt(5)\n"
    "v_mfma_f32_16x16x32_bf16 v[104:107], v[160:163], a[56:59], v[104:107]\n"
    "buffer_load_dwordx4 v23, s[48:51], s66 offen offset:0 sc1 lds\n"
    "v_mfma_f32_16x16x32_bf16 v[108:111], v[160:163], a[60:63], v[108:111]\n"
    "ds_read_b128 v[144:147], v30 offset:38912\n"
    "v_mfma_f32_16x16x32_bf16 v[112:115], v[164:167], a[56:59], v[112:115]\n"
    "buffer_load_dwordx4 v23, s[48:51], s66 offen offset:1024 sc1 lds\n"
    "v_mfma_f32_16x16x32_bf16 v[116:119], v[164:167], a[60:63], v[116:119]\n"
    "ds_read_b128 v[148:151], v30 offset:39936\n"
    "v_mfma_f32_16x16x32_bf16 v[64:67], v[168:171], a[64:67], v[64:67]\n"
    "buffer_load_dwordx4 v23, s[48:51], s66 offen offset:2048 sc1 lds\n"
    "v_mfma_f32_16x16x32_bf16 v[68:71], v[168:171], a[68:71], v[68:71]\n"
    "ds_read_b128 v[152:155], v30 offset:40960\n"
    "s_waitcnt lgkmcnt(5)\n"
    "v_mfma_f32_16x16x32_bf16 v[72:75], v[172:175], a[64:67], v[72:75]\n"
    "s_waitcnt vmcnt(11)\n"
    "ds_read_b128 a[160:163], v28 offset:32768\n"
    "ds_read_b128 a[164:167], v28 offset:34816\n"
    "ds_read_b128 a[168:171], v29 offset:32768\n"
    "v_mfma_f32_16x16x32_bf16 v[76:79], v[172:175], a[68:71], v[76:79]\n"
    "ds_read_b128 v[156:159], v30 offset:41984\n"
    "ds_read_b128 a[172:175], v29 offset:34816\n"
    "v_mfma_f32_16x16x32_bf16 v[80:83], v[176:179], a[64:67], v[80:83]\n"
    "buffer_load_dwordx4 v23, s[48:51], s66 offen offset:3072 sc1 lds\n"
    "v_mfma_f32_16x16x32_bf16 v[84:87], v[176:179], a[68:71], v[84:87]\n"
    "ds_read_b128 v[160:163], v30 offset:43008\n"
    "s_add_u32 m0, s59, 65536\n"
    "s_add_u32 s63, s62, 1536\n"
    "v_mfma_f32_16x16x32_bf16 v[88:91], v[180:183], a[64:67], v[88:91]\n"
    "s_and_b32 s63, s63, 2047\n"
    "v_mfma_f32_16x16x32_bf16 v[92:95], v[180:183], a[68:71], v[92:95]\n"
    "ds_read_b128 v[164:167], v30 offset:44032\n"
    "s_waitcnt lgkmcnt(9)\n"
    "v_mfma_f32_16x16x32_bf16 v[64:67], v[136:139], a[72:75], v[64:67]\n"
    "buffer_load_dwordx4 v19, s[44:47], s63 offen offset:0 lds\n"
    "v_mfma_f32_16x16x32_bf16 v[68:71], v[136:139], a[76:79], v[68:71]\n"
    "ds_read_b128 v[168:171], v30 offset:45056\n"
    "v_mfma_f32_16x16x32_bf16 v[72:75], v[140:143], a[72:75], v[72:75]\n"
    "buffer_load_dwordx4 v20, s[44:47], s63 offen offset:1024 lds\n"
    "v_mfma_f32_16x16x32_bf16 v[76:79], v[140:143], a[76:79], v[76:79]\n"
    "ds_read_b128 v[172:175], v30 offset:46080\n"
    "v_mfma_f32_16x16x32_bf16 v[80:83], v[144:147], a[72:75], v[80:83]\n"
    "buffer_load_dwordx4 v21, s[44:47], s63 offen offset:2048 lds\n"
    "v_mfma_f32_16x16x32_bf16 v[84:87], v[144:147], a[76:79], v[84:87]\n"
    "ds_read_b128 v[176:179], v30 offset:47104\n"
    "s_waitcnt lgkmcnt(6)\n"
    "v_mfma_f32_16x16x32_bf16 v[88:91], v[148:151], a[72:75], v[88:91]\n"
    "buffer_load_dwordx4 v22, s[44:47], s63 offen offset:3072 lds\n"
    "v_mfma_f32_16x16x32_bf16 v[92:95], v[148:151], a[76:79], v[92:95]\n"
    "ds_read_b128 v[180:183], v30 offset:48128\n"
    "s_add_u32 m0, s59, 81920\n"
    "s_add_u32 s63, s62, 1664\n"
    "v_mfma_f32_16x16x32_bf16 v[64:67], v[152:155], a[80:83], v[64:67]\n"
    "s_and_b32 s63, s63, 2047\n"
    "v_mfma_f32_16x16x32_bf16 v[68:71], v[152:155], a[84:87], v[68:71]\n"
    "ds_read_b128 v[136:139], v32 offset:0\n"
    "v_mfma_f32_16x16x32_bf16 v[72:75], v[156:159], a[80:83], v[72:75]\n"
    "buffer_load_dwordx4 v19, s[44:47], s63 offen offset:0 lds\n"
    "v_mfma_f32_16x16x32_bf16 v[76:79], v[156:159], a[84:87], v[76:79]\n"
    "ds_read_b128 v[140:143], v32 offset:1024\n"
    "s_waitcnt lgkmcnt(5)\n"
    "v_mfma_f32_16x16x32_bf16 v[80:83], v[160:163], a[80:83], v[80:83]\n"
    "v_mfma_f32_16x16x32_bf16 v[84:87], v[160:163], a[84:87], v[84:87]\n"
    "ds_read_b128 v[144:147], v32 offset:2048\n"
    "v_mfma_f32_16x16x32_bf16 v[88:91], v[164:167], a[80:83], v[88:91]\n"
    "buffer_load_dwordx4 v20, s[44:47], s63 offen offset:1024 lds\n"
    "v_mfma_f32_16x16x32_bf16 v[92:95], v[164:167], a[84:87], v[92:95]\n"
    "ds_read_b128 v[148:151], v32 offset:4096\n"
    "v_mfma_f32_16x16x32_bf16 v[64:67], v[168:171], a[88:91], v[64:67]\n"
    "v_mfma_f32_16x16x32_bf16 v[68:71], v[168:171], a[92:95], v[68:71]\n"
    "ds_read_b128 v[152:155], v32 offset:5120\n"
    "s_waitcnt lgkmcnt(5)\n"
    "v_mfma_f32_16x16x32_bf16 v[72:75], v[172:175], a[88:91], v[72:75]\n"
    "buffer_load_dwordx4 v21, s[44:47], s63 offen offset:2048 lds\n"
    "v_mfma_f32_16x16x32_bf16 v[76:79], v[172:175], a[92:95], v[76:79]\n"
    "ds_read_b128 v[156:159], v32 offset:6144\n"
    "v_mfma_f32_16x16x32_bf16 v[80:83], v[176:179], a[88:91], v[80:83]\n"
    "s_waitcnt vmcnt(15)\n"
    "ds_read_b128 a[176:179], v28 offset:49152\n"
    "ds_read_b128 a[180:183], v28 offset:51200\n"
    "ds_read_b128 a[184:187], v29 offset:49152\n"
    "v_mfma_f32_16x16x32_bf16 v[84:87], v[176:179], a[92:95], v[84:87]\n"
    "ds_read_b128 v[160:163], v32 offset:8192\n"
    "ds_read_b128 a[188:191], v29 offset:51200\n"
    "v_mfma_f32_16x16x32_bf16 v[88:91], v[180:183], a[88:91], v[88:91]\n"
    "buffer_load_dwordx4 v22, s[44:47], s63 offen offset:3072 lds\n"
    "v_mfma_f32_16x16x32_bf16 v[92:95], v[180:183], a[92:95], v[92:95]\n"
    "ds_read_b128 v[164:167], v32 offset:9216\n"
    "s_add_u32 m0, s59, 16384\n"
    "s_waitcnt lgkmcnt(9)\n"
    "v_mfma_f32_16x16x32_bf16 v[96:99], v[136:139], a[64:67], v[96:99]\n"
    "s_add_u32 s67, s69, 20\n"
    "s_and_b32 s67, s67, 31\n"
    "s_mul_i32 s66, s67, 4096\n"
    "v_mfma_f32_16x16x32_bf16 v[100:103], v[136:139], a[68:71], v[100:103]\n"
    "ds_read_b128 v[168:171], v32 offset:10240\n"
    "s_add_u32 s66, s66, s68\n"
    "v_mfma_f32_16x16x32_bf16 v[104:107], v[140:143], a[64:67], v[104:107]\n"
    "buffer_load_dwordx4 v23, s[48:51], s66 offen offset:0 sc1 lds\n"
    "v_mfma_f32_16x16x32_bf16 v[108:111], v[140:143], a[68:71], v[108:111]\n"
    "ds_read_b128 v[172:175], v32 offset:12288\n"
    "v_mfma_f32_16x16x32_bf16 v[112:115], v[144:147], a[64:67], v[112:115]\n"
    "v_mfma_f32_16x16x32_bf16 v[116:119], v[144:147], a[68:71], v[116:119]\n"
    "ds_read_b128 v[176:179], v32 offset:13312\n"
    "s_waitcnt lgkmcnt(9)\n"
    "v_mfma_f32_16x16x32_bf16 v[96:99], v[148:151], a[72:75], v[96:99]\n"
    "buffer_load_dwordx4 v23, s[48:51], s66 offen offset:1024 sc1 lds\n"
    "v_mfma_f32_16x16x32_bf16 v[100:103], v[148:151], a[76:79], v[100:103]\n"
    "ds_read_b128 v[180:183], v32 offset:14336\n"
    "v_mfma_f32_16x16x32_bf16 v[104:107], v[152:155], a[72:75], v[104:107]\n"
    "v_mfma_f32_16x16x32_bf16 v[108:111], v[152:155], a[76:79], v[108:111]\n"
    "s_waitcnt vmcnt(10)\n"
    "s_barrier\n"
    "ds_read_b128 v[136:139], v30 offset:49152\n"
    "v_mfma_f32_16x16x32_bf16 v[112:115], v[156:159], a[72:75], v[112:115]\n"
    "buffer_load_dwordx4 v23, s[48:51], s66 offen offset:2048 sc1 lds\n"
    "v_mfma_f32_16x16x32_bf16 v[116:119], v[156:159], a[76:79], v[116:119]\n"
    "ds_read_b128 v[140:143], v30 offset:50176\n"
    "s_waitcnt lgkmcnt(5)\n"
    "v_mfma_f32_16x16x32_bf16 v[96:99], v[160:163], a[80:83], v[96:99]\n"
    "v_mfma_f32_16x16x32_bf16 v[100:103], v[160:163], a[84:87], v[100:103]\n"
    "ds_read_b128 v[144:147], v30 offset:51200\n"
    "v_mfma_f32_16x16x32_bf16 v[104:107], v[164:167], a[80:83], v[104:107]\n"
    "buffer_load_dwordx4 v23, s[48:51], s66 offen offset:3072 sc1 lds\n"
    "v_mfma_f32_16x16x32_bf16 v[108:111], v[164:167], a[84:87], v[108:111]\n"
    "ds_read_b128 v[148:151], v30 offset:52224\n"
    "s_add_u32 m0, s59, 0\n"
    "s_add_u32 s67, s69, 24\n"
    "v_mfma_f32_16x16x32_bf16 v[112:115], v[168:171], a[80:83], v[112:115]\n"
    "s_and_b32 s67, s67, 31\n"
    "s_mul_i32 s66, s67, 4096\n"
    "s_add_u32 s66, s66, s68\n"
    "v_mfma_f32_16x16x32_bf16 v[116:119], v[168:171], a[84:87], v[116:119]\n"
    "ds_read_b128 v[152:155], v30 offset:53248\n"
    "s_waitcnt lgkmcnt(5)\n"
    "v_mfma_f32_16x16x32_bf16 v[96:99], v[172:175], a[88:91], v[96:99]\n"
    "buffer_load_dwordx4 v23, s[48:51], s66 offen offset:0 sc1 lds\n"
    "v_mfma_f32_16x16x32_bf16 v[100:103], v[172:175], a[92:95], v[100:103]\n"
    "ds_read_b128 v[156:159], v30 offset:54272\n"
    "v_mfma_f32_16x16x32_bf16 v[104:107], v[176:179], a[88:91], v[104:107]\n"
    "v_mfma_f32_16x16x32_bf16 v[108:111], v[176:179], a[92:95], v[108:111]\n"
    "ds_read_b128 v[160:163], v30 offset:55296\n"
    "v_mfma_f32_16x16x32_bf16 v[112:115], v[180:183], a[88:91], v[112:115]\n"
    "s_waitcnt vmcnt(9)\n"
    "ds_read_b128 a[192:195], v28 offset:0\n"
    "ds_read_b128 a[196:199], v28 offset:2048\n"
    "ds_read_b128 a[200:203], v29 offset:0\n"
    "v_mfma_f32_16x16x32_bf16 v[116:119], v[180:183], a[92:95], v[116:119]\n"
    "ds_read_b128 v[164:167], v30 offset:56320\n"
    "ds_read_b128 a[204:207], v29 offset:2048\n"
    "s_waitcnt lgkmcnt(9)\n"
    "v_mfma_f32_16x16x32_bf16 v[64:67], v[136:139], a[96:99], v[64:67]\n"
    "buffer_load_dwordx4 v23, s[48:51], s66 offen offset:1024 sc1 lds\n"
    "v_mfma_f32_16x16x32_bf16 v[68:71], v[136:139], a[100:103], v[68:71]\n"
    "ds_read_b128 v[168:171], v30 offset:57344\n"
    "v_mfma_f32_16x16x32_bf16 v[72:75], v[140:143], a[96:99], v[72:75]\n"
    "buffer_load_dwordx4 v23, s[48:51], s66 offen offset:2048 sc1 lds\n"
    "v_mfma_f32_16x16x32_bf16 v[76:79], v[140:143], a[100:103], v[76:79]\n"
    "ds_read_b128 v[172:175], v30 offset:58368\n"
    "v_mfma_f32_16x16x32_bf16 v[80:83], v[144:147], a[96:99], v[80:83]\n"
    "buffer_load_dwordx4 v23, s[48:51], s66 offen offset:3072 sc1 lds\n"
    "v_mfma_f32_16x16x32_bf16 v[84:87], v[144:147], a[100:103], v[84:87]\n"
    "ds_read_b128 v[176:179], v30 offset:59392\n"
    "s_add_u32 m0, s59, 98304\n"
    "s_waitcnt lgkmcnt(9)\n"
    "v_mfma_f32_16x16x32_bf16 v[88:91], v[148:151], a[96:99], v[88:91]\n"
    "s_add_u32 s63, s62, 1792\n"
    "s_and_b32 s63, s63, 2047\n"
    "v_mfma_f32_16x16x32_bf16 v[92:95], v[148:151], a[100:103], v[92:95]\n"
    "ds_read_b128 v[180:183], v30 offset:60416\n"
    "v_mfma_f32_16x16x32_bf16 v[64:67], v[152:155], a[104:107], v[64:67]\n"
    "buffer_load_dwordx4 v19, s[44:47], s63 offen offset:0 lds\n"
    "v_mfma_f32_16x16x32_bf16 v[68:71], v[152:155], a[108:111], v[68:71]\n"
    "ds_read_b128 v[136:139], v30 offset:61440\n"
    "v_mfma_f32_16x16x32_bf16 v[72:75], v[156:159], a[104:107], v[72:75]\n"
    "buffer_load_dwordx4 v20, s[44:47], s63 offen offset:1024 lds\n"
    "v_mfma_f32_16x16x32_bf16 v[76:79], v[156:159], a[108:111], v[76:79]\n"
    "ds_read_b128 v[140:143], v30 offset:62464\n"
    "s_waitcnt lgkmcnt(5)\n"
    "v_mfma_f32_16x16x32_bf16 v[80:83], v[160:163], a[104:107], v[80:83]\n"
    "buffer_load_dwordx4 v21, s[44:47], s63 offen offset:2048 lds\n"
    "v_mfma_f32_16x16x32_bf16 v[84:87], v[160:163], a[108:111], v[84:87]\n"
    "ds_read_b128 v[144:147], v30 offset:63488\n"
    "v_mfma_f32_16x16x32_bf16 v[88:91], v[164:167], a[104:107], v[88:91]\n"
    "buffer_load_dwordx4 v22, s[44:47], s63 offen offset:3072 lds\n"
    "v_mfma_f32_16x16x32_bf16 v[92:95], v[164:167], a[108:111], v[92:95]\n"
    "ds_read_b128 v[148:151], v30 offset:64512\n"
    "s_add_u32 m0, s59, 65536\n"
    "s_add_u32 s63, s62, 1920\n"
    "v_mfma_f32_16x16x32_bf16 v[64:67], v[168:171], a[112:115], v[64:67]\n"
    "s_and_b32 s63, s63, 2047\n"
    "v_mfma_f32_16x16x32_bf16 v[68:71], v[168:171], a[116:119], v[68:71]\n"
    "ds_read_b128 v[152:155], v32 offset:16384\n"
    "s_waitcnt lgkmcnt(5)\n"
    "v_mfma_f32_16x16x32_bf16 v[72:75], v[172:175], a[112:115], v[72:75]\n"
    "buffer_load_dwordx4 v19, s[44:47], s63 offen offset:0 lds\n"
    "v_mfma_f32_16x16x32_bf16 v[76:79], v[172:175], a[116:119], v[76:79]\n"
    "ds_read_b128 v[156:159], v32 offset:17408\n"
    "v_mfma_f32_16x16x32_bf16 v[80:83], v[176:179], a[112:115], v[80:83]\n"
    "v_mfma_f32_16x16x32_bf16 v[84:87], v[176:179], a[116:119], v[84:87]\n"
    "ds_read_b128 v[160:163], v32 offset:18432\n"
    "v_mfma_f32_16x16x32_bf16 v[88:91], v[180:183], a[112:115], v[88:91]\n"
    "buffer_load_dwordx4 v20, s[44:47], s63 offen offset:1024 lds\n"
    "v_mfma_f32_16x16x32_bf16 v[92:95], v[180:183], a[116:119], v[92:95]\n"
    "ds_read_b128 v[164:167], v32 offset:19456\n"
    "s_waitcnt lgkmcnt(5)\n"
    "v_mfma_f32_16x16x32_bf16 v[64:67], v[136:139], a[120:123], v[64:67]\n"
    "s_waitcnt vmcnt(14)\n"
    "ds_read_b128 a[208:211], v28 offset:16384\n"
    "ds_read_b128 a[212:215], v28 offset:18432\n"
    "ds_read_b128 a[216:219], v29 offset:16384\n"
    "v_mfma_f32_16x16x32_bf16 v[68:71], v[136:139], a[124:127], v[68:71]\n"
    "ds_read_b128 v[168:171], v32 offset:20480\n"
    "ds_read_b128 a[220:223], v29 offset:18432\n"
    "v_mfma_f32_16x16x32_bf16 v[72:75], v[140:143], a[120:123], v[72:75]\n"
    "buffer_load_dwordx4 v21, s[44:47], s63 offen offset:2048 lds\n"
    "v_mfma_f32_16x16x32_bf16 v[76:79], v[140:143], a[124:127], v[76:79]\n"
    "ds_read_b128 v[172:175], v32 offset:21504\n"
    "v_mfma_f32_16x16x32_bf16 v[80:83], v[144:147], a[120:123], v[80:83]\n"
    "v_mfma_f32_16x16x32_bf16 v[84:87], v[144:147], a[124:127], v[84:87]\n"
    "ds_read_b128 v[176:179], v32 offset:22528\n"
    "s_waitcnt lgkmcnt(9)\n"
    "v_mfma_f32_16x16x32_bf16 v[88:91], v[148:151], a[120:123], v[88:91]\n"
    "buffer_load_dwordx4 v22, s[44:47], s63 offen offset:3072 lds\n"
    "v_mfma_f32_16x16x32_bf16 v[92:95], v[148:151], a[124:127], v[92:95]\n"
    "ds_read_b128 v[180:183], v32 offset:23552\n"
    "s_add_u32 m0, s59, 32768\n"
    "s_add_u32 s67, s69, 28\n"
    "v_mfma_f32_16x16x32_bf16 v[64:67], v[152:155], a[128:131], v[64:67]\n"
    "s_and_b32 s67, s67, 31\n"
    "s_mul_i32 s66, s67, 4096\n"
    "s_add_u32 s66, s66, s68\n"
    "v_mfma_f32_16x16x32_bf16 v[68:71], v[152:155], a[132:135], v[68:71]\n"
    "ds_read_b128 v[136:139], v32 offset:24576\n"
    "v_mfma_f32_16x16x32_bf16 v[72:75], v[156:159], a[128:131], v[72:75]\n"
    "buffer_load_dwordx4 v23, s[48:51], s66 offen offset:0 sc1 lds\n"
    "v_mfma_f32_16x16x32_bf16 v[76:79], v[156:159], a[132:135], v[76:79]\n"
    "ds_read_b128 v[140:143], v32 offset:25600\n"
    "s_waitcnt lgkmcnt(6)\n"
    "v_mfma_f32_16x16x32_bf16 v[80:83], v[160:163], a[128:131], v[80:83]\n"
    "v_mfma_f32_16x16x32_bf16 v[84:87], v[160:163], a[132:135], v[84:87]\n"
    "ds_read_b128 v[144:147], v32 offset:26624\n"
    "v_mfma_f32_16x16x32_bf16 v[88:91], v[164:167], a[128:131], v[88:91]\n"
    "buffer_load_dwordx4 v23, s[48:51], s66 offen offset:1024 sc1 lds\n"
    "v_mfma_f32_16x16x32_bf16 v[92:95], v[164:167], a[132:135], v[92:95]\n"
    "ds_read_b128 v[148:151], v32 offset:27648\n"
    "v_mfma_f32_16x16x32_bf16 v[64:67], v[168:171], a[136:139], v[64:67]\n"
    "v_mfma_f32_16x16x32_bf16 v[68:71], v[168:171], a[140:143], v[68:71]\n"
    "ds_read_b128 v[152:155], v32 offset:28672\n"
    "s_waitcnt lgkmcnt(5)\n"
    "v_mfma_f32_16x16x32_bf16 v[72:75], v[172:175], a[136:139], v[72:75]\n"
    "buffer_load_dwordx4 v23, s[48:51], s66 offen offset:2048 sc1 lds\n"
    "v_mfma_f32_16x16x32_bf16 v[76:79], v[172:175], a[140:143], v[76:79]\n"
    "ds_read_b128 v[156:159], v32 offset:29696\n"
    "v_mfma_f32_16x16x32_bf16 v[80:83], v[176:179], a[136:139], v[80:83]\n"
    "v_mfma_f32_16x16x32_bf16 v[84:87], v[176:179], a[140:143], v[84:87]\n"
    "ds_read_b128 v[160:163], v32 offset:30720\n"
    "v_mfma_f32_16x16x32_bf16 v[88:91], v[180:183], a[136:139], v[88:91]\n"
    "buffer_load_dwordx4 v23, s[48:51], s66 offen offset:3072 sc1 lds\n"
    "v_mfma_f32_16x16x32_bf16 v[92:95], v[180:183], a[140:143], v[92:95]\n"
    "ds_read_b128 v[164:167], v32 offset:31744\n"
    "s_add_u32 m0, s59, 81920\n"
    "s_waitcnt lgkmcnt(5)\n"
    "v_mfma_f32_16x16x32_bf16 v[64:67], v[136:139], a[144:147], v[64:67]\n"
    "s_add_u32 s67, s69, 12\n"
    "s_and_b32 s67, s67, 31\n"
    "s_mul_i32 s66, s67, 3072\n"
    "v_mfma_f32_16x16x32_bf16 v[68:71], v[136:139], a[148:151], v[68:71]\n"
    "s_waitcnt vmcnt(12)\n"
    "s_barrier\n"
    "ds_read_b128 v[168:171], v30 offset:16384\n"
    "v_mfma_f32_16x16x32_bf16 v[72:75], v[140:143], a[144:147], v[72:75]\n"
    "s_waitcnt vmcnt(8)\n"
    "ds_read_b128 a[224:227], v28 offset:32768\n"
    "ds_read_b128 a[228:231], v28 offset:34816\n"
    "ds_read_b128 a[232:235], v29 offset:32768\n"
    "v_mfma_f32_16x16x32_bf16 v[76:79], v[140:143], a[148:151], v[76:79]\n"
    "ds_read_b128 v[172:175], v30 offset:17408\n"
    "ds_read_b128 a[236:239], v29 offset:34816\n"
    "s_add_u32 s66, s66, s68\n"
    "v_mfma_f32_16x16x32_bf16 v[80:83], v[144:147], a[144:147], v[80:83]\n"
    "s_add_u32 s66, s66, 131072\n"
    "v_mfma_f32_16x16x32_bf16 v[84:87], v[144:147], a[148:151], v[84:87]\n"
    "ds_read_b128 v[176:179], v30 offset:18432\n"
    "s_waitcnt lgkmcnt(9)\n"
    "v_mfma_f32_16x16x32_bf16 v[88:91], v[148:151], a[144:147], v[88:91]\n"
    "buffer_load_dwordx4 v23, s[48:51], s66 offen offset:0 sc1 lds\n"
    "v_mfma_f32_16x16x32_bf16 v[92:95], v[148:151], a[148:151], v[92:95]\n"
    "ds_read_b128 v[180:183], v30 offset:19456\n"
    "v_mfma_f32_16x16x32_bf16 v[64:67], v[152:155], a[152:155], v[64:67]\n"
    "v_mfma_f32_16x16x32_bf16 v[68:71], v[152:155], a[156:159], v[68:71]\n"
    "ds_read_b128 v[136:139], v30 offset:20480\n"
    "v_mfma_f32_16x16x32_bf16 v[72:75], v[156:159], a[152:155], v[72:75]\n"
    "buffer_load_dwordx4 v23, s[48:51], s66 offen offset:1024 sc1 lds\n"
    "v_mfma_f32_16x16x32_bf16 v[76:79], v[156:159], a[156:159], v[76:79]\n"
    "ds_read_b128 v[140:143], v30 offset:21504\n"
    "s_waitcnt lgkmcnt(9)\n"
    "v_mfma_f32_16x16x32_bf16 v[80:83], v[160:163], a[152:155], v[80:83]\n"
    "v_mfma_f32_16x16x32_bf16 v[84:87], v[160:163], a[156:159], v[84:87]\n"
    "ds_read_b128 v[144:147], v30 offset:22528\n"
    "v_mfma_f32_16x16x32_bf16 v[88:91], v[164:167], a[152:155], v[88:91]\n"
    "buffer_load_dwordx4 v23, s[48:51], s66 offen offset:2048 sc1 lds\n"
    "v_mfma_f32_16x16x32_bf16 v[92:95], v[164:167], a[156:159], v[92:95]\n"
    "ds_read_b128 v[148:151], v30 offset:23552\n"
    "s_add_u32 m0, s59, 131072\n"
    "s_add_u32 s67, s69, 16\n"
    "v_mfma_f32_16x16x32_bf16 v[64:67], v[168:171], a[160:163], v[64:67]\n"
    "s_and_b32 s67, s67, 31\n"
    "s_mul_i32 s66, s67, 3072\n"
    "s_add_u32 s66, s66, s68\n"
    "v_mfma_f32_16x16x32_bf16 v[68:71], v[168:171], a[164:167], v[68:71]\n"
    "ds_read_b128 v[152:155], v30 offset:24576\n"
    "s_add_u32 s66, s66, 131072\n"
    "s_waitcnt lgkmcnt(5)\n"
    "v_mfma_f32_16x16x32_bf16 v[72:75], v[172:175], a[160:163], v[72:75]\n"
    "buffer_load_dwordx4 v23, s[48:51], s66 offen offset:0 sc1 lds\n"
    "v_mfma_f32_16x16x32_bf16 v[76:79], v[172:175], a[164:167], v[76:79]\n"
    "ds_read_b128 v[156:159], v30 offset:25600\n"
    "v_mfma_f32_16x16x32_bf16 v[80:83], v[176:179], a[160:163], v[80:83]\n"
    "v_mfma_f32_16x16x32_bf16 v[84:87], v[176:179], a[164:167], v[84:87]\n"
    "ds_read_b128 v[160:163], v30 offset:26624\n"
    "v_mfma_f32_16x16x32_bf16 v[88:91], v[180:183], a[160:163], v[88:91]\n"
    "buffer_load_dwordx4 v23, s[48:51], s66 offen offset:1024 sc1 lds\n"
    "v_mfma_f32_16x16x32_bf16 v[92:95], v[180:183], a[164:167], v[92:95]\n"
    "ds_read_b128 v[164:167], v30 offset:27648\n"
    "s_waitcnt lgkmcnt(5)\n"
    "v_mfma_f32_16x16x32_bf16 v[64:67], v[136:139], a[168:171], v[64:67]\n"
    "v_mfma_f32_16x16x32_bf16 v[68:71], v[136:139], a[172:175], v[68:71]\n"
    "ds_read_b128 v[168:171], v30 offset:28672\n"
    "v_mfma_f32_16x16x32_bf16 v[72:75], v[140:143], a[168:171], v[72:75]\n"
    "buffer_load_dwordx4 v23, s[48:51], s66 offen offset:2048 sc1 lds\n"
    "v_mfma_f32_16x16x32_bf16 v[76:79], v[140:143], a[172:175], v[76:79]\n"
    "ds_read_b128 v[172:175], v30 offset:29696\n"
    "s_add_u32 m0, s59, 49152\n"
    "s_add_u32 s67, s69, 20\n"
    "v_mfma_f32_16x16x32_bf16 v[80:83], v[144:147], a[168:171], v[80:83]\n"
    "s_waitcnt vmcnt(10)\n"
    "ds_read_b128 a[240:243], v28 offset:0\n"
    "ds_read_b128 a[244:247], v28 offset:2048\n"
    "ds_read_b128 a[248:251], v29 offset:0\n"
    "v_mfma_f32_16x16x32_bf16 v[84:87], v[144:147], a[172:175], v[84:87]\n"
    "ds_read_b128 v[176:179], v30 offset:30720\n"
    "ds_read_b128 a[252:255], v29 offset:2048\n"
    "s_waitcnt lgkmcnt(9)\n"
    "v_mfma_f32_16x16x32_bf16 v[88:91], v[148:151], a[168:171], v[88:91]\n"
    "s_and_b32 s67, s67, 31\n"
    "s_mul_i32 s66, s67, 3072\n"
    "s_add_u32 s66, s66, s68\n"
    "v_mfma_f32_16x16x32_bf16 v[92:95], v[148:151], a[172:175], v[92:95]\n"
    "ds_read_b128 v[180:183], v30 offset:31744\n"
    "s_add_u32 s66, s66, 131072\n"
    "v_mfma_f32_16x16x32_bf16 v[64:67], v[152:155], a[176:179], v[64:67]\n"
    "buffer_load_dwordx4 v23, s[48:51], s66 offen offset:0 sc1 lds\n"
    "v_mfma_f32_16x16x32_bf16 v[68:71], v[152:155], a[180:183], v[68:71]\n"
    "ds_read_b128 v[136:139], v30 offset:0\n"
    "v_mfma_f32_16x16x32_bf16 v[72:75], v[156:159], a[176:179], v[72:75]\n"
    "buffer_load_dwordx4 v23, s[48:51], s66 offen offset:1024 sc1 lds\n"
    "v_mfma_f32_16x16x32_bf16 v[76:79], v[156:159], a[180:183], v[76:79]\n"
    "ds_read_b128 v[140:143], v30 offset:1024\n"
    "s_waitcnt lgkmcnt(9)\n"
    "v_mfma_f32_16x16x32_bf16 v[80:83], v[160:163], a[176:179], v[80:83]\n"
    "buffer_load_dwordx4 v23, s[48:51], s66 offen offset:2048 sc1 lds\n"
    "v_mfma_f32_16x16x32_bf16 v[84:87], v[160:163], a[180:183], v[84:87]\n"
    "ds_read_b128 v[144:147], v30 offset:2048\n"
    "s_add_u32 m0, s59, 98304\n"
    "s_add_u32 s67, s69, 24\n"
    "v_mfma_f32_16x16x32_bf16 v[88:91], v[164:167], a[176:179], v[88:91]\n"
    "s_and_b32 s67, s67, 31\n"
    "s_mul_i32 s66, s67, 3072\n"
    "s_add_u32 s66, s66, s68\n"
    "v_mfma_f32_16x16x32_bf16 v[92:95], v[164:167], a[180:183], v[92:95]\n"
    "ds_read_b128 v[148:151], v30 offset:3072\n"
    "s_add_u32 s66, s66, 131072\n"
    "v_mfma_f32_16x16x32_bf16 v[64:67], v[168:171], a[184:187], v[64:67]\n"
    "v_mfma_f32_16x16x32_bf16 v[68:71], v[168:171], a[188:191], v[68:71]\n"
    "ds_read_b128 v[152:155], v30 offset:4096\n"
    "s_waitcnt lgkmcnt(5)\n"
    "v_mfma_f32_16x16x32_bf16 v[72:75], v[172:175], a[184:187], v[72:75]\n"
    "buffer_load_dwordx4 v23, s[48:51], s66 offen offset:0 sc1 lds\n"
    "v_mfma_f32_16x16x32_bf16 v[76:79], v[172:175], a[188:191], v[76:79]\n"
    "ds_read_b128 v[156:159], v30 offset:5120\n"
    "v_mfma_f32_16x16x32_bf16 v[80:83], v[176:179], a[184:187], v[80:83]\n"
    "v_mfma_f32_16x16x32_bf16 v[84:87], v[176:179], a[188:191], v[84:87]\n"
    "ds_read_b128 v[160:163], v30 offset:6144\n"
    "v_mfma_f32_16x16x32_bf16 v[88:91], v[180:183], a[184:187], v[88:91]\n"
    "v_mfma_f32_16x16x32_bf16 v[92:95], v[180:183], a[188:191], v[92:95]\n"
    "ds_read_b128 v[164:167], v30 offset:7168\n"
    "s_waitcnt lgkmcnt(5)\n"
    "v_mfma_f32_16x16x32_bf16 v[64:67], v[136:139], a[192:195], v[64:67]\n"
    "buffer_load_dwordx4 v23, s[48:51], s66 offen offset:1024 sc1 lds\n"
    "v_mfma_f32_16x16x32_bf16 v[68:71], v[136:139], a[196:199], v[68:71]\n"
    "ds_read_b128 v[168:171], v30 offset:8192\n"
    "v_mfma_f32_16x16x32_bf16 v[72:75], v[140:143], a[192:195], v[72:75]\n"
    "v_mfma_f32_16x16x32_bf16 v[76:79], v[140:143], a[196:199], v[76:79]\n"
    "ds_read_b128 v[172:175], v30 offset:9216\n"
    "v_mfma_f32_16x16x32_bf16 v[80:83], v[144:147], a[192:195], v[80:83]\n"
    "v_mfma_f32_16x16x32_bf16 v[84:87], v[144:147], a[196:199], v[84:87]\n"
    "ds_read_b128 v[176:179], v30 offset:10240\n"
    "s_waitcnt lgkmcnt(5)\n"
    "v_mfma_f32_16x16x32_bf16 v[88:91], v[148:151], a[192:195], v[88:91]\n"
    "buffer_load_dwordx4 v23, s[48:51], s66 offen offset:2048 sc1 lds\n"
    "v_mfma_f32_16x16x32_bf16 v[92:95], v[148:151], a[196:199], v[92:95]\n"
    "ds_read_b128 v[180:183], v30 offset:11264\n"
    "s_add_u32 m0, s59, 65536\n"
    "s_add_u32 s67, s69, 28\n"
    "v_mfma_f32_16x16x32_bf16 v[64:67], v[152:155], a[200:203], v[64:67]\n"
    "s_and_b32 s67, s67, 31\n"
    "s_mul_i32 s66, s67, 3072\n"
    "s_add_u32 s66, s66, s68\n"
    "v_mfma_f32_16x16x32_bf16 v[68:71], v[152:155], a[204:207], v[68:71]\n"
    "ds_read_b128 v[136:139], v30 offset:12288\n"
    "s_add_u32 s66, s66, 131072\n"
    "v_mfma_f32_16x16x32_bf16 v[72:75], v[156:159], a[200:203], v[72:75]\n"
    "v_mfma_f32_16x16x32_bf16 v[76:79], v[156:159], a[204:207], v[76:79]\n"
    "ds_read_b128 v[140:143], v30 offset:13312\n"
    "s_waitcnt lgkmcnt(5)\n"
    "v_mfma_f32_16x16x32_bf16 v[80:83], v[160:163], a[200:203], v[80:83]\n"
    "buffer_load_dwordx4 v23, s[48:51], s66 offen offset:0 sc1 lds\n"
    "v_mfma_f32_16x16x32_bf16 v[84:87], v[160:163], a[204:207], v[84:87]\n"
    "ds_read_b128 v[144:147], v30 offset:14336\n"
    "v_mfma_f32_16x16x32_bf16 v[88:91], v[164:167], a[200:203], v[88:91]\n"
    "v_mfma_f32_16x16x32_bf16 v[92:95], v[164:167], a[204:207], v[92:95]\n"
    "ds_read_b128 v[148:151], v30 offset:15360\n"
    "v_mfma_f32_16x16x32_bf16 v[64:67], v[168:171], a[208:211], v[64:67]\n"
    "v_mfma_f32_16x16x32_bf16 v[68:71], v[168:171], a[212:215], v[68:71]\n"
    "s_waitcnt vmcnt(10)\n"
    "s_barrier\n"
    "ds_read_b128 v[152:155], v30 offset:32768\n"
    "s_waitcnt lgkmcnt(5)\n"
    "v_mfma_f32_16x16x32_bf16 v[72:75], v[172:175], a[208:211], v[72:75]\n"
    "buffer_load_dwordx4 v23, s[48:51], s66 offen offset:1024 sc1 lds\n"
    "v_mfma_f32_16x16x32_bf16 v[76:79], v[172:175], a[212:215], v[76:79]\n"
    "ds_read_b128 v[156:159], v30 offset:33792\n"
    "v_mfma_f32_16x16x32_bf16 v[80:83], v[176:179], a[208:211], v[80:83]\n"
    "v_mfma_f32_16x16x32_bf16 v[84:87], v[176:179], a[212:215], v[84:87]\n"
    "ds_read_b128 v[160:163], v30 offset:36864\n"
    "v_mfma_f32_16x16x32_bf16 v[88:91], v[180:183], a[208:211], v[88:91]\n"
    "v_mfma_f32_16x16x32_bf16 v[92:95], v[180:183], a[212:215], v[92:95]\n"
    "ds_read_b128 v[164:167], v30 offset:37888\n"
    "s_waitcnt lgkmcnt(5)\n"
    "v_mfma_f32_16x16x32_bf16 v[64:67], v[136:139], a[216:219], v[64:67]\n"
    "buffer_load_dwordx4 v23, s[48:51], s66 offen offset:2048 sc1 lds\n"
    "v_mfma_f32_16x16x32_bf16 v[68:71], v[136:139], a[220:223], v[68:71]\n"
    "ds_read_b128 v[168:171], v30 offset:40960\n"
    "s_add_u32 m0, s59, 147456\n"
    "s_add_u32 s67, s56, 0\n"
    "v_mfma_f32_16x16x32_bf16 v[72:75], v[140:143], a[216:219], v[72:75]\n"
    "buffer_store_dwordx4 v[60:63], v23, s[48:51], s13 offen offset:0 sc1\n"
    "v_mfma_f32_16x16x32_bf16 v[76:79], v[140:143], a[220:223], v[76:79]\n"
    "ds_read_b128 v[172:175], v30 offset:41984\n"
    "s_and_b32 s67, s67, 31\n"
    "s_mul_i32 s66, s67, 2048\n"
    "v_mfma_f32_16x16x32_bf16 v[80:83], v[144:147], a[216:219], v[80:83]\n"
    "buffer_store_dwordx4 v[60:63], v23, s[48:51], s13 offen offset:1024 sc1\n"
    "v_mfma_f32_16x16x32_bf16 v[84:87], v[144:147], a[220:223], v[84:87]\n"
    "ds_read_b128 v[176:179], v30 offset:45056\n"
    "s_add_u32 s66, s66, s68\n"
    "s_waitcnt lgkmcnt(5)\n"
    "v_mfma_f32_16x16x32_bf16 v[88:91], v[148:151], a[216:219], v[88:91]\n"
    "buffer_store_dwordx4 v[60:63], v23, s[48:51], s14 offen sc1\n"
    "v_mfma_f32_16x16x32_bf16 v[92:95], v[148:151], a[220:223], v[92:95]\n"
    "ds_read_b128 v[180:183], v30 offset:46080\n"
    "v_mfma_f32_16x16x32_bf16 v[64:67], v[152:155], a[224:227], v[64:67]\n"
    "s_add_u32 s66, s66, 229376\n"
    "v_mfma_f32_16x16x32_bf16 v[68:71], v[152:155], a[228:231], v[68:71]\n"
    "ds_read_b128 v[136:139], v30 offset:34816\n"
    "v_mfma_f32_16x16x32_bf16 v[72:75], v[156:159], a[224:227], v[72:75]\n"
    "v_mfma_f32_16x16x32_bf16 v[76:79], v[156:159], a[228:231], v[76:79]\n"
    "ds_read_b128 v[140:143], v30 offset:35840\n"
    "s_waitcnt lgkmcnt(5)\n"
    "v_mfma_f32_16x16x32_bf16 v[64:67], v[160:163], a[232:235], v[64:67]\n"
    "buffer_load_dwordx4 v23, s[48:51], s66 offen offset:0 sc1 lds\n"
    "v_mfma_f32_16x16x32_bf16 v[68:71], v[160:163], a[236:239], v[68:71]\n"
    "ds_read_b128 v[144:147], v30 offset:38912\n"
    "v_mfma_f32_16x16x32_bf16 v[72:75], v[164:167], a[232:235], v[72:75]\n"
    "v_mfma_f32_16x16x32_bf16 v[76:79], v[164:167], a[236:239], v[76:79]\n"
    "ds_read_b128 v[148:151], v30 offset:39936\n"
    "v_mfma_f32_16x16x32_bf16 v[64:67], v[168:171], a[240:243], v[64:67]\n"
    "v_mfma_f32_16x16x32_bf16 v[68:71], v[168:171], a[244:247], v[68:71]\n"
    "ds_read_b128 v[152:155], v30 offset:43008\n"
    "s_waitcnt lgkmcnt(5)\n"
    "v_mfma_f32_16x16x32_bf16 v[72:75], v[172:175], a[240:243], v[72:75]\n"
    "buffer_load_dwordx4 v23, s[48:51], s66 offen offset:1024 sc1 lds\n"
    "v_mfma_f32_16x16x32_bf16 v[76:79], v[172:175], a[244:247], v[76:79]\n"
    "ds_read_b128 v[156:159], v30 offset:44032\n"
    "v_mfma_f32_16x16x32_bf16 v[64:67], v[176:179], a[248:251], v[64:67]\n"
    "v_mfma_f32_16x16x32_bf16 v[68:71], v[176:179], a[252:255], v[68:71]\n"
    "ds_read_b128 v[160:163], v30 offset:47104\n"
    "v_mfma_f32_16x16x32_bf16 v[72:75], v[180:183], a[248:251], v[72:75]\n"
    "v_mfma_f32_16x16x32_bf16 v[76:79], v[180:183], a[252:255], v[76:79]\n"
    "ds_read_b128 v[164:167], v30 offset:48128\n"
    "s_waitcnt lgkmcnt(5)\n"
    "v_mfma_f32_16x16x32_bf16 v[80:83], v[136:139], a[224:227], v[80:83]\n"
    "buffer_load_dwordx4 v23, s[48:51], s66 offen offset:2048 sc1 lds\n"
    "v_mfma_f32_16x16x32_bf16 v[84:87], v[136:139], a[228:231], v[84:87]\n"
    "ds_read_b128 v[168:171], v31 offset:16384\n"
    "v_mfma_f32_16x16x32_bf16 v[88:91], v[140:143], a[224:227], v[88:91]\n"
    "v_mfma_f32_16x16x32_bf16 v[92:95], v[140:143], a[228:231], v[92:95]\n"
    "ds_read_b128 v[172:175], v31 offset:17408\n"
    "v_mfma_f32_16x16x32_bf16 v[80:83], v[144:147], a[232:235], v[80:83]\n"
    "v_cvt_pk_bf16_f32 v200, v64, v65\n"
    "v_cvt_pk_bf16_f32 v201, v66, v67\n"
    "v_mfma_f32_16x16x32_bf16 v[84:87], v[144:147], a[236:239], v[84:87]\n"
    "ds_read_b128 v[176:179], v31 offset:18432\n"
    "s_waitcnt lgkmcnt(5)\n"
    "v_mfma_f32_16x16x32_bf16 v[88:91], v[148:151], a[232:235], v[88:91]\n"
    "v_cvt_pk_bf16_f32 v202, v72, v73\n"
    "v_cvt_pk_bf16_f32 v203, v74, v75\n"
    "v_mfma_f32_16x16x32_bf16 v[92:95], v[148:151], a[236:239], v[92:95]\n"
    "ds_read_b128 v[180:183], v31 offset:20480\n"
    "v_mfma_f32_16x16x32_bf16 v[80:83], v[152:155], a[240:243], v[80:83]\n"
    "buffer_store_dwordx4 v[200:203], v23, s[48:51], s11 offen offset:0 sc1\n"
    "v_mfma_f32_16x16x32_bf16 v[84:87], v[152:155], a[244:247], v[84:87]\n"
    "ds_read_b128 v[136:139], v31 offset:21504\n"
    "v_cvt_pk_bf16_f32 v204, v68, v69\n"
    "v_mfma_f32_16x16x32_bf16 v[88:91], v[156:159], a[240:243], v[88:91]\n"
    "v_cvt_pk_bf16_f32 v205, v70, v71\n"
    "v_cvt_pk_bf16_f32 v206, v76, v77\n"
    "v_mfma_f32_16x16x32_bf16 v[92:95], v[156:159], a[244:247], v[92:95]\n"
    "ds_read_b128 v[140:143], v31 offset:22528\n"
    "s_waitcnt lgkmcnt(5)\n"
    "v_mfma_f32_16x16x32_bf16 v[80:83], v[160:163], a[248:251], v[80:83]\n"
    "v_cvt_pk_bf16_f32 v207, v78, v79\n"
    "v_mfma_f32_16x16x32_bf16 v[84:87], v[160:163], a[252:255], v[84:87]\n"
    "ds_read_b128 v[144:147], v31 offset:24576\n"
    "v_mfma_f32_16x16x32_bf16 v[88:91], v[164:167], a[248:251], v[88:91]\n"
    "buffer_store_dwordx4 v[204:207], v23, s[48:51], s11 offen offset:1024 sc1\n"
    "v_mfma_f32_16x16x32_bf16 v[92:95], v[164:167], a[252:255], v[92:95]\n"
    "ds_read_b128 v[148:151], v31 offset:25600\n"
    "v_mfma_f32_16x16x32_bf16 v[96:99], v[168:171], a[96:99], v[96:99]\n"
    "v_mfma_f32_16x16x32_bf16 v[100:103], v[168:171], a[100:103], v[100:103]\n"
    "ds_read_b128 v[152:155], v31 offset:26624\n"
    "s_waitcnt lgkmcnt(5)\n"
    "v_mfma_f32_16x16x32_bf16 v[104:107], v[172:175], a[96:99], v[104:107]\n"
    "buffer_load_dwordx4 v23, s[48:51], s66 offen offset:3072 sc1 lds\n"
    "v_mfma_f32_16x16x32_bf16 v[108:111], v[172:175], a[100:103], v[108:111]\n"
    "ds_read_b128 v[156:159], v31 offset:28672\n"
    "s_add_u32 m0, s59, 16384\n"
    "s_add_u32 s67, s56, 8\n"
    "v_mfma_f32_16x16x32_bf16 v[112:115], v[176:179], a[96:99], v[112:115]\n"
    "v_cvt_pk_bf16_f32 v208, v80, v81\n"
    "v_cvt_pk_bf16_f32 v209, v82, v83\n"
    "v_mfma_f32_16x16x32_bf16 v[116:119], v[176:179], a[100:103], v[116:119]\n"
    "ds_read_b128 v[160:163], v31 offset:29696\n"
    "v_cvt_pk_bf16_f32 v210, v88, v89\n"
    "v_mfma_f32_16x16x32_bf16 v[96:99], v[180:183], a[104:107], v[96:99]\n"
    "v_cvt_pk_bf16_f32 v211, v90, v91\n"
    "s_and_b32 s67, s67, 31\n"
    "v_mfma_f32_16x16x32_bf16 v[100:103], v[180:183], a[108:111], v[100:103]\n"
    "ds_read_b128 v[164:167], v31 offset:30720\n"
    "s_mul_i32 s66, s67, 2048\n"
    "s_waitcnt lgkmcnt(5)\n"
    "v_mfma_f32_16x16x32_bf16 v[104:107], v[136:139], a[104:107], v[104:107]\n"
    "buffer_store_dwordx4 v[208:211], v23, s[48:51], s12 offen offset:0 sc1\n"
    "v_mfma_f32_16x16x32_bf16 v[108:111], v[136:139], a[108:111], v[108:111]\n"
    "s_waitcnt vmcnt(16)\n"
    "s_barrier\n"
    "ds_read_b128 v[168:171], v32 offset:0\n"
    "v_mfma_f32_16x16x32_bf16 v[112:115], v[140:143], a[104:107], v[112:115]\n"
    "v_cvt_pk_bf16_f32 v212, v84, v85\n"
    "v_cvt_pk_bf16_f32 v213, v86, v87\n"
    "v_mfma_f32_16x16x32_bf16 v[116:119], v[140:143], a[108:111], v[116:119]\n"
    "ds_read_b128 v[172:175], v32 offset:1024\n"
    "v_cvt_pk_bf16_f32 v214, v92, v93\n"
    "v_mfma_f32_16x16x32_bf16 v[96:99], v[144:147], a[112:115], v[96:99]\n"
    "v_cvt_pk_bf16_f32 v215, v94, v95\n"
    "s_add_u32 s66, s66, s68\n"
    "v_mfma_f32_16x16x32_bf16 v[100:103], v[144:147], a[116:119], v[100:103]\n"
    "ds_read_b128 v[176:179], v32 offset:2048\n"
    "s_add_u32 s66, s66, 229376\n"
    "s_waitcnt lgkmcnt(5)\n"
    "v_mfma_f32_16x16x32_bf16 v[104:107], v[148:151], a[112:115], v[104:107]\n"
    "buffer_store_dwordx4 v[212:215], v23, s[48:51], s12 offen offset:1024 sc1\n"
    "v_mfma_f32_16x16x32_bf16 v[108:111], v[148:151], a[116:119], v[108:111]\n"
    "ds_read_b128 v[180:183], v32 offset:4096\n"
    "v_mfma_f32_16x16x32_bf16 v[112:115], v[152:155], a[112:115], v[112:115]\n"
    "buffer_load_dwordx4 v23, s[48:51], s66 offen offset:0 sc1 lds\n"
    "v_mfma_f32_16x16x32_bf16 v[116:119], v[152:155], a[116:119], v[116:119]\n"
    "ds_read_b128 v[136:139], v32 offset:5120\n"
    "v_mfma_f32_16x16x32_bf16 v[96:99], v[156:159], a[120:123], v[96:99]\n"
    "buffer_load_dwordx4 v23, s[48:51], s66 offen offset:1024 sc1 lds\n"
    "v_mfma_f32_16x16x32_bf16 v[100:103], v[156:159], a[124:127], v[100:103]\n"
    "ds_read_b128 v[140:143], v32 offset:6144\n"
    "s_waitcnt lgkmcnt(5)\n"
    "v_mfma_f32_16x16x32_bf16 v[104:107], v[160:163], a[120:123], v[104:107]\n"
    "buffer_load_dwordx4 v23, s[48:51], s66 offen offset:2048 sc1 lds\n"
    "v_mfma_f32_16x16x32_bf16 v[108:111], v[160:163], a[124:127], v[108:111]\n"
    "ds_read_b128 v[144:147], v32 offset:8192\n"
    "v_mfma_f32_16x16x32_bf16 v[112:115], v[164:167], a[120:123], v[112:115]\n"
    "buffer_load_dwordx4 v23, s[48:51], s66 offen offset:3072 sc1 lds\n"
    "v_mfma_f32_16x16x32_bf16 v[116:119], v[164:167], a[124:127], v[116:119]\n"
    "ds_read_b128 v[148:151], v32 offset:9216\n"
    "s_add_u32 m0, s59, 0\n"
    "s_add_u32 s67, s56, 16\n"
    "v_mfma_f32_16x16x32_bf16 v[96:99], v[168:171], a[128:131], v[96:99]\n"
    "s_and_b32 s67, s67, 31\n"
    "s_mul_i32 s66, s67, 2048\n"
    "s_add_u32 s66, s66, s68\n"
    "v_mfma_f32_16x16x32_bf16 v[100:103], v[168:171], a[132:135], v[100:103]\n"
    "ds_read_b128 v[152:155], v32 offset:10240\n"
    "s_add_u32 s66, s66, 229376\n"
    "s_waitcnt lgkmcnt(5)\n"
    "v_mfma_f32_16x16x32_bf16 v[104:107], v[172:175], a[128:131], v[104:107]\n"
    "v_mfma_f32_16x16x32_bf16 v[108:111], v[172:175], a[132:135], v[108:111]\n"
    "ds_read_b128 v[156:159], v32 offset:12288\n"
    "v_mfma_f32_16x16x32_bf16 v[112:115], v[176:179], a[128:131], v[112:115]\n"
    "buffer_load_dwordx4 v23, s[48:51], s66 offen offset:0 sc1 lds\n"
    "v_mfma_f32_16x16x32_bf16 v[116:119], v[176:179], a[132:135], v[116:119]\n"
    "ds_read_b128 v[160:163], v32 offset:13312\n"
    "v_mfma_f32_16x16x32_bf16 v[96:99], v[180:183], a[136:139], v[96:99]\n"
    "v_mfma_f32_16x16x32_bf16 v[100:103], v[180:183], a[140:143], v[100:103]\n"
    "ds_read_b128 v[164:167], v32 offset:14336\n"
    "s_waitcnt lgkmcnt(5)\n"
    "v_mfma_f32_16x16x32_bf16 v[104:107], v[136:139], a[136:139], v[104:107]\n"
    "v_mfma_f32_16x16x32_bf16 v[108:111], v[136:139], a[140:143], v[108:111]\n"
    "ds_read_b128 v[168:171], v30 offset:49152\n"
    "v_mfma_f32_16x16x32_bf16 v[112:115], v[140:143], a[136:139], v[112:115]\n"
    "buffer_load_dwordx4 v23, s[48:51], s66 offen offset:1024 sc1 lds\n"
    "v_mfma_f32_16x16x32_bf16 v[116:119], v[140:143], a[140:143], v[116:119]\n"
    "ds_read_b128 v[172:175], v30 offset:50176\n"
    "v_mfma_f32_16x16x32_bf16 v[96:99], v[144:147], a[144:147], v[96:99]\n"
    "v_mfma_f32_16x16x32_bf16 v[100:103], v[144:147], a[148:151], v[100:103]\n"
    "ds_read_b128 v[176:179], v30 offset:51200\n"
    "s_waitcnt lgkmcnt(5)\n"
    "v_mfma_f32_16x16x32_bf16 v[104:107], v[148:151], a[144:147], v[104:107]\n"
    "v_mfma_f32_16x16x32_bf16 v[108:111], v[148:151], a[148:151], v[108:111]\n"
    "ds_read_b128 v[180:183], v30 offset:53248\n"
    "v_mfma_f32_16x16x32_bf16 v[112:115], v[152:155], a[144:147], v[112:115]\n"
    "buffer_load_dwordx4 v23, s[48:51], s66 offen offset:2048 sc1 lds\n"
    "v_mfma_f32_16x16x32_bf16 v[116:119], v[152:155], a[148:151], v[116:119]\n"
    "ds_read_b128 v[136:139], v30 offset:54272\n"
    "v_mfma_f32_16x16x32_bf16 v[96:99], v[156:159], a[152:155], v[96:99]\n"
    "v_mfma_f32_16x16x32_bf16 v[100:103], v[156:159], a[156:159], v[100:103]\n"
    "ds_read_b128 v[140:143], v30 offset:55296\n"
    "s_waitcnt lgkmcnt(5)\n"
    "v_mfma_f32_16x16x32_bf16 v[104:107], v[160:163], a[152:155], v[104:107]\n"
    "v_mfma_f32_16x16x32_bf16 v[108:111], v[160:163], a[156:159], v[108:111]\n"
    "ds_read_b128 v[144:147], v30 offset:57344\n"
    "v_mfma_f32_16x16x32_bf16 v[112:115], v[164:167], a[152:155], v[112:115]\n"
    "buffer_load_dwordx4 v23, s[48:51], s66 offen offset:3072 sc1 lds\n"
    "v_mfma_f32_16x16x32_bf16 v[116:119], v[164:167], a[156:159], v[116:119]\n"
    "ds_read_b128 v[148:151], v30 offset:58368\n"
    "s_add_u32 m0, s59, 32768\n"
    "s_add_u32 s67, s56, 24\n"
    "v_mfma_f32_16x16x32_bf16 v[96:99], v[168:171], a[160:163], v[96:99]\n"
    "s_and_b32 s67, s67, 31\n"
    "s_mul_i32 s66, s67, 2048\n"
    "s_add_u32 s66, s66, s68\n"
    "v_mfma_f32_16x16x32_bf16 v[100:103], v[168:171], a[164:167], v[100:103]\n"
    "ds_read_b128 v[152:155], v30 offset:59392\n"
    "s_add_u32 s66, s66, 229376\n"
    "s_waitcnt lgkmcnt(5)\n"
    "v_mfma_f32_16x16x32_bf16 v[104:107], v[172:175], a[160:163], v[104:107]\n"
    "v_mfma_f32_16x16x32_bf16 v[108:111], v[172:175], a[164:167], v[108:111]\n"
    "ds_read_b128 v[156:159], v30 offset:61440\n"
    "v_mfma_f32_16x16x32_bf16 v[112:115], v[176:179], a[160:163], v[112:115]\n"
    "buffer_load_dwordx4 v23, s[48:51], s66 offen offset:0 sc1 lds\n"
    "v_mfma_f32_16x16x32_bf16 v[116:119], v[176:179], a[164:167], v[116:119]\n"
    "ds_read_b128 v[160:163], v30 offset:62464\n"
    "v_mfma_f32_16x16x32_bf16 v[96:99], v[180:183], a[168:171], v[96:99]\n"
    "v_mfma_f32_16x16x32_bf16 v[100:103], v[180:183], a[172:175], v[100:103]\n"
    "ds_read_b128 v[164:167], v30 offset:63488\n"
    "s_waitcnt lgkmcnt(5)\n"
    "v_mfma_f32_16x16x32_bf16 v[104:107], v[136:139], a[168:171], v[104:107]\n"
    "v_mfma_f32_16x16x32_bf16 v[108:111], v[136:139], a[172:175], v[108:111]\n"
    "s_waitcnt vmcnt(20)\n"
    "s_barrier\n"
    "ds_read_b128 v[168:171], v31 offset:32768\n"
    "v_mfma_f32_16x16x32_bf16 v[112:115], v[140:143], a[168:171], v[112:115]\n"
    "buffer_load_dwordx4 v23, s[48:51], s66 offen offset:1024 sc1 lds\n"
    "v_mfma_f32_16x16x32_bf16 v[116:119], v[140:143], a[172:175], v[116:119]\n"
    "ds_read_b128 v[172:175], v31 offset:33792\n"
    "v_mfma_f32_16x16x32_bf16 v[96:99], v[144:147], a[176:179], v[96:99]\n"
    "v_mfma_f32_16x16x32_bf16 v[100:103], v[144:147], a[180:183], v[100:103]\n"
    "ds_read_b128 v[176:179], v31 offset:34816\n"
    "s_waitcnt lgkmcnt(5)\n"
    "v_mfma_f32_16x16x32_bf16 v[104:107], v[148:151], a[176:179], v[104:107]\n"
    "v_mfma_f32_16x16x32_bf16 v[108:111], v[148:151], a[180:183], v[108:111]\n"
    "ds_read_b128 v[180:183], v31 offset:36864\n"
    "v_mfma_f32_16x16x32_bf16 v[112:115], v[152:155], a[176:179], v[112:115]\n"
    "buffer_load_dwordx4 v23, s[48:51], s66 offen offset:2048 sc1 lds\n"
    "v_mfma_f32_16x16x32_bf16 v[116:119], v[152:155], a[180:183], v[116:119]\n"
    "ds_read_b128 v[136:139], v31 offset:37888\n"
    "v_mfma_f32_16x16x32_bf16 v[96:99], v[156:159], a[184:187], v[96:99]\n"
    "v_mfma_f32_16x16x32_bf16 v[100:103], v[156:159], a[188:191], v[100:103]\n"
    "ds_read_b128 v[140:143], v31 offset:38912\n"
    "s_waitcnt lgkmcnt(5)\n"
    "v_mfma_f32_16x16x32_bf16 v[104:107], v[160:163], a[184:187], v[104:107]\n"
    "v_mfma_f32_16x16x32_bf16 v[108:111], v[160:163], a[188:191], v[108:111]\n"
    "ds_read_b128 v[144:147], v31 offset:40960\n"
    "v_mfma_f32_16x16x32_bf16 v[112:115], v[164:167], a[184:187], v[112:115]\n"
    "buffer_load_dwordx4 v23, s[48:51], s66 offen offset:3072 sc1 lds\n"
    "v_mfma_f32_16x16x32_bf16 v[116:119], v[164:167], a[188:191], v[116:119]\n"
    "ds_read_b128 v[148:151], v31 offset:41984\n"
    "v_mfma_f32_16x16x32_bf16 v[96:99], v[168:171], a[192:195], v[96:99]\n"
    "v_mfma_f32_16x16x32_bf16 v[100:103], v[168:171], a[196:199], v[100:103]\n"
    "ds_read_b128 v[152:155], v31 offset:43008\n"
    "s_waitcnt lgkmcnt(5)\n"
    "v_mfma_f32_16x16x32_bf16 v[104:107], v[172:175], a[192:195], v[104:107]\n"
    "v_mfma_f32_16x16x32_bf16 v[108:111], v[172:175], a[196:199], v[108:111]\n"
    "ds_read_b128 v[156:159], v31 offset:45056\n"
    "v_mfma_f32_16x16x32_bf16 v[112:115], v[176:179], a[192:195], v[112:115]\n"
    "v_mfma_f32_16x16x32_bf16 v[116:119], v[176:179], a[196:199], v[116:119]\n"
    "ds_read_b128 v[160:163], v31 offset:46080\n"
    "v_mfma_f32_16x16x32_bf16 v[96:99], v[180:183], a[200:203], v[96:99]\n"
    "v_mfma_f32_16x16x32_bf16 v[100:103], v[180:183], a[204:207], v[100:103]\n"
    "ds_read_b128 v[164:167], v31 offset:47104\n"
    "s_waitcnt lgkmcnt(5)\n"
    "v_mfma_f32_16x16x32_bf16 v[104:107], v[136:139], a[200:203], v[104:107]\n"
    "v_mfma_f32_16x16x32_bf16 v[108:111], v[136:139], a[204:207], v[108:111]\n"
    "ds_read_b128 v[168:171], v31 offset:0\n"
    "v_mfma_f32_16x16x32_bf16 v[112:115], v[140:143], a[200:203], v[112:115]\n"
    "v_mfma_f32_16x16x32_bf16 v[116:119], v[140:143], a[204:207], v[116:119]\n"
    "ds_read_b128 v[172:175], v31 offset:1024\n"
    "v_mfma_f32_16x16x32_bf16 v[96:99], v[144:147], a[208:211], v[96:99]\n"
    "v_mfma_f32_16x16x32_bf16 v[100:103], v[144:147], a[212:215], v[100:103]\n"
    "ds_read_b128 v[176:179], v31 offset:4096\n"
    "s_waitcnt lgkmcnt(5)\n"
    "v_mfma_f32_16x16x32_bf16 v[104:107], v[148:151], a[208:211], v[104:107]\n"
    "v_mfma_f32_16x16x32_bf16 v[108:111], v[148:151], a[212:215], v[108:111]\n"
    "ds_read_b128 v[180:183], v31 offset:5120\n"
    "v_mfma_f32_16x16x32_bf16 v[112:115], v[152:155], a[208:211], v[112:115]\n"
    "v_mfma_f32_16x16x32_bf16 v[116:119], v[152:155], a[212:215], v[116:119]\n"
    "ds_read_b128 v[136:139], v31 offset:8192\n"
    "v_mfma_f32_16x16x32_bf16 v[96:99], v[156:159], a[216:219], v[96:99]\n"
    "v_mfma_f32_16x16x32_bf16 v[100:103], v[156:159], a[220:223], v[100:103]\n"
    "ds_read_b128 v[140:143], v31 offset:9216\n"
    "s_waitcnt lgkmcnt(5)\n"
    "v_mfma_f32_16x16x32_bf16 v[104:107], v[160:163], a[216:219], v[104:107]\n"
    "v_mfma_f32_16x16x32_bf16 v[108:111], v[160:163], a[220:223], v[108:111]\n"
    "ds_read_b128 v[144:147], v31 offset:12288\n"
    "v_mfma_f32_16x16x32_bf16 v[112:115], v[164:167], a[216:219], v[112:115]\n"
    "v_mfma_f32_16x16x32_bf16 v[116:119], v[164:167], a[220:223], v[116:119]\n"
    "ds_read_b128 v[148:151], v31 offset:13312\n"
    "v_mfma_f32_16x16x32_bf16 v[96:99], v[168:171], a[224:227], v[96:99]\n"
    "v_mfma_f32_16x16x32_bf16 v[100:103], v[168:171], a[228:231], v[100:103]\n"
    "ds_read_b128 v[152:155], v31 offset:2048\n"
    "s_waitcnt lgkmcnt(5)\n"
    "v_mfma_f32_16x16x32_bf16 v[104:107], v[172:175], a[224:227], v[104:107]\n"
    "v_mfma_f32_16x16x32_bf16 v[108:111], v[172:175], a[228:231], v[108:111]\n"
    "ds_read_b128 v[156:159], v31 offset:6144\n"
    "s_waitcnt vmcnt(12)\n"
    "s_mov_b64 exec, 1\n"
    "buffer_store_dwordx2 v[38:39], v45, s[48:51], s29 offen offset:0 sc1\n"
    "s_mov_b64 exec, -1\n"
    "v_mfma_f32_16x16x32_bf16 v[96:99], v[176:179], a[232:235], v[96:99]\n"
    "v_mfma_f32_16x16x32_bf16 v[100:103], v[176:179], a[236:239], v[100:103]\n"
    "ds_read_b128 v[160:163], v31 offset:10240\n"
    "v_mfma_f32_16x16x32_bf16 v[104:107], v[180:183], a[232:235], v[104:107]\n"
    "v_mfma_f32_16x16x32_bf16 v[108:111], v[180:183], a[236:239], v[108:111]\n"
    "ds_read_b128 v[164:167], v31 offset:14336\n"
    "s_waitcnt lgkmcnt(5)\n"
    "v_mfma_f32_16x16x32_bf16 v[96:99], v[136:139], a[240:243], v[96:99]\n"
    "v_mfma_f32_16x16x32_bf16 v[100:103], v[136:139], a[244:247], v[100:103]\n"
    "s_waitcnt vmcnt(9)\n"
    "s_barrier\n"
    "ds_read_b128 v[168:171], v32 offset:16384\n"
    "v_mfma_f32_16x16x32_bf16 v[104:107], v[140:143], a[240:243], v[104:107]\n"
    "v_mfma_f32_16x16x32_bf16 v[108:111], v[140:143], a[244:247], v[108:111]\n"
    "ds_read_b128 v[172:175], v32 offset:17408\n"
    "v_mfma_f32_16x16x32_bf16 v[96:99], v[144:147], a[248:251], v[96:99]\n"
    "v_mfma_f32_16x16x32_bf16 v[100:103], v[144:147], a[252:255], v[100:103]\n"
    "ds_read_b128 v[176:179], v32 offset:18432\n"
    "s_waitcnt lgkmcnt(5)\n"
    "v_mfma_f32_16x16x32_bf16 v[104:107], v[148:151], a[248:251], v[104:107]\n"
    "v_mfma_f32_16x16x32_bf16 v[108:111], v[148:151], a[252:255], v[108:111]\n"
    "ds_read_b128 v[180:183], v32 offset:19456\n"
    "v_mfma_f32_16x16x32_bf16 v[112:115], v[152:155], a[224:227], v[112:115]\n"
    "v_mfma_f32_16x16x32_bf16 v[116:119], v[152:155], a[228:231], v[116:119]\n"
    "ds_read_b128 v[136:139], v32 offset:20480\n"
    "v_mfma_f32_16x16x32_bf16 v[112:115], v[156:159], a[232:235], v[112:115]\n"
    "v_mfma_f32_16x16x32_bf16 v[116:119], v[156:159], a[236:239], v[116:119]\n"
    "ds_read_b128 v[140:143], v32 offset:21504\n"
    "s_waitcnt lgkmcnt(5)\n"
    "v_mfma_f32_16x16x32_bf16 v[112:115], v[160:163], a[240:243], v[112:115]\n"
    "v_cvt_pk_bf16_f32 v216, v96, v97\n"
    "v_cvt_pk_bf16_f32 v217, v98, v99\n"
    "v_mfma_f32_16x16x32_bf16 v[116:119], v[160:163], a[244:247], v[116:119]\n"
    "ds_read_b128 v[144:147], v32 offset:22528\n"
    "v_cvt_pk_bf16_f32 v218, v104, v105\n"
    "v_mfma_f32_16x16x32_bf16 v[112:115], v[164:167], a[248:251], v[112:115]\n"
    "v_cvt_pk_bf16_f32 v219, v106, v107\n"
    "v_mfma_f32_16x16x32_bf16 v[116:119], v[164:167], a[252:255], v[116:119]\n"
    "ds_read_b128 v[148:151], v32 offset:23552\n"
    "v_mfma_f32_16x16x32_bf16 v[120:123], v[168:171], a[0:3], 0\n"
    "buffer_store_dwordx4 v[216:219], v23, s[48:51], s13 offen offset:0 sc1\n"
    "v_mfma_f32_16x16x32_bf16 v[124:127], v[168:171], a[4:7], 0\n"
    "ds_read_b128 v[152:155], v32 offset:24576\n"
    "s_waitcnt lgkmcnt(5)\n"
    "v_mfma_f32_16x16x32_bf16 v[128:131], v[172:175], a[0:3], 0\n"
    "v_cvt_pk_bf16_f32 v220, v100, v101\n"
    "v_cvt_pk_bf16_f32 v221, v102, v103\n"
    "v_mfma_f32_16x16x32_bf16 v[132:135], v[172:175], a[4:7], 0\n"
    "ds_read_b128 v[156:159], v32 offset:25600\n"
    "v_cvt_pk_bf16_f32 v222, v108, v109\n"
    "v_mfma_f32_16x16x32_bf16 v[120:123], v[176:179], a[8:11], v[120:123]\n"
    "v_cvt_pk_bf16_f32 v223, v110, v111\n"
    "v_mfma_f32_16x16x32_bf16 v[124:127], v[176:179], a[12:15], v[124:127]\n"
    "ds_read_b128 v[160:163], v32 offset:26624\n"
    "v_mfma_f32_16x16x32_bf16 v[128:131], v[180:183], a[8:11], v[128:131]\n"
    "buffer_store_dwordx4 v[220:223], v23, s[48:51], s13 offen offset:1024 sc1\n"
    "v_mfma_f32_16x16x32_bf16 v[132:135], v[180:183], a[12:15], v[132:135]\n"
    "ds_read_b128 v[164:167], v32 offset:27648\n"
    "s_waitcnt lgkmcnt(5)\n"
    "v_mfma_f32_16x16x32_bf16 v[120:123], v[136:139], a[16:19], v[120:123]\n"
    "v_cvt_pk_bf16_f32 v224, v112, v113\n"
    "v_cvt_pk_bf16_f32 v225, v114, v115\n"
    "v_mfma_f32_16x16x32_bf16 v[124:127], v[136:139], a[20:23], v[124:127]\n"
    "ds_read_b128 v[168:171], v32 offset:28672\n"
    "v_cvt_pk_bf16_f32 v226, v116, v117\n"
    "v_mfma_f32_16x16x32_bf16 v[128:131], v[140:143], a[16:19], v[128:131]\n"
    "v_cvt_pk_bf16_f32 v227, v118, v119\n"
    "v_mfma_f32_16x16x32_bf16 v[132:135], v[140:143], a[20:23], v[132:135]\n"
    "ds_read_b128 v[172:175], v32 offset:29696\n"
    "v_mfma_f32_16x16x32_bf16 v[120:123], v[144:147], a[24:27], v[120:123]\n"
    "buffer_store_dwordx4 v[224:227], v23, s[48:51], s14 offen sc1\n"
    "v_mfma_f32_16x16x32_bf16 v[124:127], v[144:147], a[28:31], v[124:127]\n"
    "ds_read_b128 v[176:179], v32 offset:30720\n"
    "s_waitcnt lgkmcnt(5)\n"
    "v_mfma_f32_16x16x32_bf16 v[128:131], v[148:151], a[24:27], v[128:131]\n"
    "v_mfma_f32_16x16x32_bf16 v[132:135], v[148:151], a[28:31], v[132:135]\n"
    "ds_read_b128 v[180:183], v32 offset:31744\n"
    "v_mfma_f32_16x16x32_bf16 v[120:123], v[152:155], a[32:35], v[120:123]\n"
    "v_mfma_f32_16x16x32_bf16 v[124:127], v[152:155], a[36:39], v[124:127]\n"
    "ds_read_b128 v[136:139], v30 offset:16384\n"
    "v_mfma_f32_16x16x32_bf16 v[128:131], v[156:159], a[32:35], v[128:131]\n"
    "v_mfma_f32_16x16x32_bf16 v[132:135], v[156:159], a[36:39], v[132:135]\n"
    "ds_read_b128 v[140:143], v30 offset:17408\n"
    "s_waitcnt lgkmcnt(5)\n"
    "v_mfma_f32_16x16x32_bf16 v[120:123], v[160:163], a[40:43], v[120:123]\n"
    "v_mfma_f32_16x16x32_bf16 v[124:127], v[160:163], a[44:47], v[124:127]\n"
    "ds_read_b128 v[144:147], v30 offset:18432\n"
    "v_mfma_f32_16x16x32_bf16 v[128:131], v[164:167], a[40:43], v[128:131]\n"
    "v_mfma_f32_16x16x32_bf16 v[132:135], v[164:167], a[44:47], v[132:135]\n"
    "ds_read_b128 v[148:151], v30 offset:19456\n"
    "v_mfma_f32_16x16x32_bf16 v[120:123], v[168:171], a[48:51], v[120:123]\n"
    "v_mfma_f32_16x16x32_bf16 v[124:127], v[168:171], a[52:55], v[124:127]\n"
    "ds_read_b128 v[152:155], v30 offset:20480\n"
    "s_waitcnt lgkmcnt(5)\n"
    "v_mfma_f32_16x16x32_bf16 v[128:131], v[172:175], a[48:51], v[128:131]\n"
    "v_mfma_f32_16x16x32_bf16 v[132:135], v[172:175], a[52:55], v[132:135]\n"
    "ds_read_b128 v[156:159], v30 offset:21504\n"
    "v_mfma_f32_16x16x32_bf16 v[120:123], v[176:179], a[56:59], v[120:123]\n"
    "v_mfma_f32_16x16x32_bf16 v[124:127], v[176:179], a[60:63], v[124:127]\n"
    "ds_read_b128 v[160:163], v30 offset:22528\n"
    "buffer_load_dwordx2 v[40:41], v44, s[48:51], s21 offen offset:0 sc1\n"
    "v_mfma_f32_16x16x32_bf16 v[128:131], v[180:183], a[56:59], v[128:131]\n"
    "v_mfma_f32_16x16x32_bf16 v[132:135], v[180:183], a[60:63], v[132:135]\n"
    "ds_read_b128 v[164:167], v30 offset:23552\n"
    "s_waitcnt lgkmcnt(5)\n"
    "v_mfma_f32_16x16x32_bf16 v[120:123], v[136:139], a[64:67], v[120:123]\n"
    "v_mfma_f32_16x16x32_bf16 v[124:127], v[136:139], a[68:71], v[124:127]\n"
    "ds_read_b128 v[168:171], v30 offset:24576\n"
    "v_mfma_f32_16x16x32_bf16 v[128:131], v[140:143], a[64:67], v[128:131]\n"
    "v_mfma_f32_16x16x32_bf16 v[132:135], v[140:143], a[68:71], v[132:135]\n"
    "ds_read_b128 v[172:175], v30 offset:25600\n"
    "v_mfma_f32_16x16x32_bf16 v[120:123], v[144:147], a[72:75], v[120:123]\n"
    "v_mfma_f32_16x16x32_bf16 v[124:127], v[144:147], a[76:79], v[124:127]\n"
    "ds_read_b128 v[176:179], v30 offset:26624\n"
    "s_waitcnt lgkmcnt(5)\n"
    "v_mfma_f32_16x16x32_bf16 v[128:131], v[148:151], a[72:75], v[128:131]\n"
    "v_mfma_f32_16x16x32_bf16 v[132:135], v[148:151], a[76:79], v[132:135]\n"
    "ds_read_b128 v[180:183], v30 offset:27648\n"
    "v_mfma_f32_16x16x32_bf16 v[120:123], v[152:155], a[80:83], v[120:123]\n"
    "v_mfma_f32_16x16x32_bf16 v[124:127], v[152:155], a[84:87], v[124:127]\n"
    "ds_read_b128 v[136:139], v30 offset:28672\n"
    "v_mfma_f32_16x16x32_bf16 v[128:131], v[156:159], a[80:83], v[128:131]\n"
    "v_mfma_f32_16x16x32_bf16 v[132:135], v[156:159], a[84:87], v[132:135]\n"
    "ds_read_b128 v[140:143], v30 offset:29696\n"
    "s_waitcnt lgkmcnt(5)\n"
    "v_mfma_f32_16x16x32_bf16 v[120:123], v[160:163], a[88:91], v[120:123]\n"
    "v_mfma_f32_16x16x32_bf16 v[124:127], v[160:163], a[92:95], v[124:127]\n"
    "ds_read_b128 v[144:147], v30 offset:30720\n"
    "v_mfma_f32_16x16x32_bf16 v[128:131], v[164:167], a[88:91], v[128:131]\n"
    "v_mfma_f32_16x16x32_bf16 v[132:135], v[164:167], a[92:95], v[132:135]\n"
    "ds_read_b128 v[148:151], v30 offset:31744\n"
    "v_mfma_f32_16x16x32_bf16 v[120:123], v[168:171], a[96:99], v[120:123]\n"
    "v_mfma_f32_16x16x32_bf16 v[124:127], v[168:171], a[100:103], v[124:127]\n"
    "s_waitcnt vmcnt(5)\n"
    "s_barrier\n"
    "ds_read_b128 v[152:155], v30 offset:0\n"
    "s_waitcnt lgkmcnt(5)\n"
    "v_mfma_f32_16x16x32_bf16 v[128:131], v[172:175], a[96:99], v[128:131]\n"
    "v_mfma_f32_16x16x32_bf16 v[132:135], v[172:175], a[100:103], v[132:135]\n"
    "ds_read_b128 v[156:159], v30 offset:1024\n"
    "v_mfma_f32_16x16x32_bf16 v[120:123], v[176:179], a[104:107], v[120:123]\n"
    "v_mfma_f32_16x16x32_bf16 v[124:127], v[176:179], a[108:111], v[124:127]\n"
    "ds_read_b128 v[160:163], v30 offset:2048\n"
    "v_mfma_f32_16x16x32_bf16 v[128:131], v[180:183], a[104:107], v[128:131]\n"
    "v_mfma_f32_16x16x32_bf16 v[132:135], v[180:183], a[108:111], v[132:135]\n"
    "ds_read_b128 v[164:167], v30 offset:3072\n"
    "s_waitcnt lgkmcnt(5)\n"
    "v_mfma_f32_16x16x32_bf16 v[120:123], v[136:139], a[112:115], v[120:123]\n"
    "v_mfma_f32_16x16x32_bf16 v[124:127], v[136:139], a[116:119], v[124:127]\n"
    "ds_read_b128 v[168:171], v30 offset:4096\n"
    "v_mfma_f32_16x16x32_bf16 v[128:131], v[140:143], a[112:115], v[128:131]\n"
    "v_mfma_f32_16x16x32_bf16 v[132:135], v[140:143], a[116:119], v[132:135]\n"
    "ds_read_b128 v[172:175], v30 offset:5120\n"
    "v_mfma_f32_16x16x32_bf16 v[120:123], v[144:147], a[120:123], v[120:123]\n"
    "v_mfma_f32_16x16x32_bf16 v[124:127], v[144:147], a[124:127], v[124:127]\n"
    "ds_read_b128 v[176:179], v30 offset:6144\n"
    "s_waitcnt lgkmcnt(5)\n"
    "v_mfma_f32_16x16x32_bf16 v[128:131], v[148:151], a[120:123], v[128:131]\n"
    "v_mfma_f32_16x16x32_bf16 v[132:135], v[148:151], a[124:127], v[132:135]\n"
    "ds_read_b128 v[180:183], v30 offset:7168\n"
    "s_waitcnt vmcnt(0)\n"
    "v_cmp_ne_u64 vcc, v[40:41], v[38:39]\n"
    "s_cbranch_vccnz L9_%=\n"
    "L8_%=:\n"
    "v_mfma_f32_16x16x32_bf16 v[120:123], v[152:155], a[128:131], v[120:123]\n"
    "buffer_load_dwordx4 v[64:67], v23, s[48:51], s15 offen offset:0 sc1\n"
    "v_mfma_f32_16x16x32_bf16 v[124:127], v[152:155], a[132:135], v[124:127]\n"
    "ds_read_b128 v[136:139], v30 offset:8192\n"
    "v_mfma_f32_16x16x32_bf16 v[128:131], v[156:159], a[128:131], v[128:131]\n"
    "buffer_load_dwordx4 v[68:71], v23, s[48:51], s15 offen offset:1024 sc1\n"
    "v_mfma_f32_16x16x32_bf16 v[132:135], v[156:159], a[132:135], v[132:135]\n"
    "ds_read_b128 v[140:143], v30 offset:9216\n"
    "s_waitcnt lgkmcnt(5)\n"
    "v_mfma_f32_16x16x32_bf16 v[120:123], v[160:163], a[136:139], v[120:123]\n"
    "buffer_load_dwordx4 v[80:83], v23, s[48:51], s26 offen offset:0 sc1\n"
    "v_mfma_f32_16x16x32_bf16 v[124:127], v[160:163], a[140:143], v[124:127]\n"
    "ds_read_b128 v[144:147], v30 offset:10240\n"
    "v_mfma_f32_16x16x32_bf16 v[128:131], v[164:167], a[136:139], v[128:131]\n"
    "buffer_load_dwordx4 v[84:87], v23, s[48:51], s26 offen offset:1024 sc1\n"
    "v_mfma_f32_16x16x32_bf16 v[132:135], v[164:167], a[140:143], v[132:135]\n"
    "ds_read_b128 v[148:151], v30 offset:11264\n"
    "v_mfma_f32_16x16x32_bf16 v[120:123], v[168:171], a[144:147], v[120:123]\n"
    "buffer_load_dwordx4 v[232:235], v23, s[48:51], s27 offen offset:0 sc1\n"
    "v_mfma_f32_16x16x32_bf16 v[124:127], v[168:171], a[148:151], v[124:127]\n"
    "ds_read_b128 v[152:155], v30 offset:12288\n"
    "s_waitcnt lgkmcnt(5)\n"
    "v_mfma_f32_16x16x32_bf16 v[128:131], v[172:175], a[144:147], v[128:131]\n"
    "buffer_load_dwordx4 v[236:239], v23, s[48:51], s27 offen offset:1024 sc1\n"
    "v_mfma_f32_16x16x32_bf16 v[132:135], v[172:175], a[148:151], v[132:135]\n"
    "ds_read_b128 v[156:159], v30 offset:13312\n"
    "v_mfma_f32_16x16x32_bf16 v[120:123], v[176:179], a[152:155], v[120:123]\n"
    "s_mov_b64 exec, s[30:31]\n"
    "buffer_load_dwordx4 v[240:243], v23, s[48:51], s15 offen offset:2048 sc1\n"
    "s_mov_b64 exec, -1\n"
    "v_mfma_f32_16x16x32_bf16 v[124:127], v[176:179], a[156:159], v[124:127]\n"
    "ds_read_b128 v[160:163], v30 offset:14336\n"
    "v_mfma_f32_16x16x32_bf16 v[128:131], v[180:183], a[152:155], v[128:131]\n"
    "s_mov_b64 exec, s[30:31]\n"
    "buffer_load_dwordx4 v[244:247], v23, s[48:51], s26 offen offset:2048 sc1\n"
    "s_mov_b64 exec, -1\n"
    "v_mfma_f32_16x16x32_bf16 v[132:135], v[180:183], a[156:159], v[132:135]\n"
    "ds_read_b128 v[164:167], v30 offset:15360\n"
    "s_waitcnt lgkmcnt(5)\n"
    "v_mfma_f32_16x16x32_bf16 v[120:123], v[136:139], a[160:163], v[120:123]\n"
    "s_mov_b64 exec, s[30:31]\n"
    "buffer_load_dwordx4 v[248:251], v23, s[48:51], s27 offen offset:2048 sc1\n"
    "s_mov_b64 exec, -1\n"
    "v_mfma_f32_16x16x32_bf16 v[124:127], v[136:139], a[164:167], v[124:127]\n"
    "ds_read_b128 v[168:171], v30 offset:32768\n"
    "v_mfma_f32_16x16x32_bf16 v[128:131], v[140:143], a[160:163], v[128:131]\n"
    "v_mfma_f32_16x16x32_bf16 v[132:135], v[140:143], a[164:167], v[132:135]\n"
    "ds_read_b128 v[172:175], v30 offset:33792\n"
    "v_mfma_f32_16x16x32_bf16 v[120:123], v[144:147], a[168:171], v[120:123]\n"
    "v_mfma_f32_16x16x32_bf16 v[124:127], v[144:147], a[172:175], v[124:127]\n"
    "ds_read_b128 v[176:179], v30 offset:34816\n"
    "s_waitcnt lgkmcnt(5)\n"
    "v_mfma_f32_16x16x32_bf16 v[128:131], v[148:151], a[168:171], v[128:131]\n"
    "v_mfma_f32_16x16x32_bf16 v[132:135], v[148:151], a[172:175], v[132:135]\n"
    "ds_read_b128 v[180:183], v30 offset:35840\n"
    "v_mfma_f32_16x16x32_bf16 v[120:123], v[152:155], a[176:179], v[120:123]\n"
    "v_mfma_f32_16x16x32_bf16 v[124:127], v[152:155], a[180:183], v[124:127]\n"
    "ds_read_b128 v[136:139], v30 offset:36864\n"
    "v_mfma_f32_16x16x32_bf16 v[128:131], v[156:159], a[176:179], v[128:131]\n"
    "v_mfma_f32_16x16x32_bf16 v[132:135], v[156:159], a[180:183], v[132:135]\n"
    "ds_read_b128 v[140:143], v30 offset:37888\n"
    "s_waitcnt lgkmcnt(5)\n"
    "v_mfma_f32_16x16x32_bf16 v[120:123], v[160:163], a[184:187], v[120:123]\n"
    "v_mfma_f32_16x16x32_bf16 v[124:127], v[160:163], a[188:191], v[124:127]\n"
    "ds_read_b128 v[144:147], v30 offset:38912\n"
    "v_mfma_f32_16x16x32_bf16 v[128:131], v[164:167], a[184:187], v[128:131]\n"
    "v_mfma_f32_16x16x32_bf16 v[132:135], v[164:167], a[188:191], v[132:135]\n"
    "ds_read_b128 v[148:151], v30 offset:39936\n"
    "v_mfma_f32_16x16x32_bf16 v[120:123], v[168:171], a[192:195], v[120:123]\n"
    "v_mfma_f32_16x16x32_bf16 v[124:127], v[168:171], a[196:199], v[124:127]\n"
    "ds_read_b128 v[152:155], v30 offset:40960\n"
    "s_waitcnt lgkmcnt(5)\n"
    "v_mfma_f32_16x16x32_bf16 v[128:131], v[172:175], a[192:195], v[128:131]\n"
    "v_mfma_f32_16x16x32_bf16 v[132:135], v[172:175], a[196:199], v[132:135]\n"
    "ds_read_b128 v[156:159], v30 offset:41984\n"
    "v_mfma_f32_16x16x32_bf16 v[120:123], v[176:179], a[200:203], v[120:123]\n"
    "v_mfma_f32_16x16x32_bf16 v[124:127], v[176:179], a[204:207], v[124:127]\n"
    "ds_read_b128 v[160:163], v30 offset:43008\n"
    "v_mfma_f32_16x16x32_bf16 v[128:131], v[180:183], a[200:203], v[128:131]\n"
    "v_mfma_f32_16x16x32_bf16 v[132:135], v[180:183], a[204:207], v[132:135]\n"
    "ds_read_b128 v[164:167], v30 offset:44032\n"
    "s_waitcnt lgkmcnt(5)\n"
    "v_mfma_f32_16x16x32_bf16 v[120:123], v[136:139], a[208:211], v[120:123]\n"
    "v_mfma_f32_16x16x32_bf16 v[124:127], v[136:139], a[212:215], v[124:127]\n"
    "ds_read_b128 v[168:171], v30 offset:45056\n"
    "v_mfma_f32_16x16x32_bf16 v[128:131], v[140:143], a[208:211], v[128:131]\n"
    "v_mfma_f32_16x16x32_bf16 v[132:135], v[140:143], a[212:215], v[132:135]\n"
    "ds_read_b128 v[172:175], v30 offset:46080\n"
    "v_mfma_f32_16x16x32_bf16 v[120:123], v[144:147], a[216:219], v[120:123]\n"
    "v_mfma_f32_16x16x32_bf16 v[124:127], v[144:147], a[220:223], v[124:127]\n"
    "ds_read_b128 v[176:179], v30 offset:47104\n"
    "s_waitcnt lgkmcnt(5)\n"
    "v_mfma_f32_16x16x32_bf16 v[128:131], v[148:151], a[216:219], v[128:131]\n"
    "v_mfma_f32_16x16x32_bf16 v[132:135], v[148:151], a[220:223], v[132:135]\n"
    "ds_read_b128 v[180:183], v30 offset:48128\n"
    "v_mfma_f32_16x16x32_bf16 v[120:123], v[152:155], a[224:227], v[120:123]\n"
    "v_mfma_f32_16x16x32_bf16 v[124:127], v[152:155], a[228:231], v[124:127]\n"
    "v_mfma_f32_16x16x32_bf16 v[128:131], v[156:159], a[224:227], v[128:131]\n"
    "v_mfma_f32_16x16x32_bf16 v[132:135], v[156:159], a[228:231], v[132:135]\n"
    "s_waitcnt lgkmcnt(3)\n"
    "v_mfma_f32_16x16x32_bf16 v[120:123], v[160:163], a[232:235], v[120:123]\n"
    "v_mfma_f32_16x16x32_bf16 v[124:127], v[160:163], a[236:239], v[124:127]\n"
    "v_mfma_f32_16x16x32_bf16 v[128:131], v[164:167], a[232:235], v[128:131]\n"
    "v_mfma_f32_16x16x32_bf16 v[132:135], v[164:167], a[236:239], v[132:135]\n"
    "v_mfma_f32_16x16x32_bf16 v[120:123], v[168:171], a[240:243], v[120:123]\n"
    "v_mfma_f32_16x16x32_bf16 v[124:127], v[168:171], a[244:247], v[124:127]\n"
    "s_waitcnt lgkmcnt(0)\n"
    "v_mfma_f32_16x16x32_bf16 v[128:131], v[172:175], a[240:243], v[128:131]\n"
    "v_mfma_f32_16x16x32_bf16 v[132:135], v[172:175], a[244:247], v[132:135]\n"
    "v_mfma_f32_16x16x32_bf16 v[120:123], v[176:179], a[248:251], v[120:123]\n"
    "v_mfma_f32_16x16x32_bf16 v[124:127], v[176:179], a[252:255], v[124:127]\n"
    "v_mfma_f32_16x16x32_bf16 v[128:131], v[180:183], a[248:251], v[128:131]\n"
    "v_mfma_f32_16x16x32_bf16 v[132:135], v[180:183], a[252:255], v[132:135]\n"
    "s_waitcnt vmcnt(5)\n"
    "v_mfma_f32_16x16x32_bf16 v[120:123], v[48:51], v[64:67], v[120:123]\n"
    "v_mfma_f32_16x16x32_bf16 v[128:131], v[52:55], v[64:67], v[128:131]\n"
    "v_mfma_f32_16x16x32_bf16 v[124:127], v[48:51], v[68:71], v[124:127]\n"
    "v_mfma_f32_16x16x32_bf16 v[132:135], v[52:55], v[68:71], v[132:135]\n"
    "v_mfma_f32_16x16x32_bf16 v[120:123], v[184:187], v[80:83], v[120:123]\n"
    "v_mfma_f32_16x16x32_bf16 v[128:131], v[188:191], v[80:83], v[128:131]\n"
    "v_mfma_f32_16x16x32_bf16 v[124:127], v[184:187], v[84:87], v[124:127]\n"
    "v_mfma_f32_16x16x32_bf16 v[132:135], v[188:191], v[84:87], v[132:135]\n"
    "s_branch L10_%=\n"
    "L4_%=:\n"
    "v_and_b32 v2, 15, v1\n"
    "v_lshrrev_b32 v6, 1, v2\n"
    "v_xor_b32 v6, v6, v4\n"
    "v_lshlrev_b32 v6, 4, v6\n"
    "v_lshl_add_u32 v24, v2, 7, v6\n"
    "v_xor_b32 v25, 64, v24\n"
    "s_add_u32 s56, s59, 65536\n"
    "v_add_u32 v28, s56, v24\n"
    "v_add_u32 v29, s56, v25\n"
    "s_and_b32 s27, s22, 1\n"
    "s_lshl_b32 s33, s22, 10\n"
    "s_mul_i32 s26, s27, 9216\n"
    "s_lshr_b32 s34, s34, 3\n"
    "s_add_u32 s26, s26, s34\n"
    "s_add_u32 s26, s26, 589824\n"
    "s_lshr_b32 s29, s22, 1\n"
    "s_lshl_b32 s29, s29, 7\n"
    "s_add_u32 s26, s26, s29\n"
    "s_lshl_b32 s27, s27, 6\n"
    "v_xor_b32 v18, s27, v5\n"
    "v_mad_u32_u24 v18, v3, s30, v18\n"
    "v_add_u32 v18, s26, v18\n"
    "v_subrev_u32 v36, s29, v18\n"
    "s_lshl_b32 s61, s60, 1\n"
    "s_min_u32 s61, s61, 896\n"
    "s_add_u32 m0, s33, 147456\n"
    "s_nop 0\n"
    "buffer_load_dwordx4 v18, s[40:43], s61 offen offset:0 lds\n"
    "s_mov_b32 s61, s60\n"
    "s_mov_b32 s46, 8388608\n"
    "s_mov_b32 s47, 0x20000\n"
    "s_mov_b64 s[48:49], s[20:21]\n"
    "s_mov_b32 s50, 16777216\n"
    "s_mov_b32 s51, 0x20000\n"
    "s_mov_b64 s[52:53], s[18:19]\n"
    "s_mov_b32 s54, 2359296\n"
    "s_mov_b32 s55, 0x20000\n"
    "s_lshl_b32 s26, s23, 2\n"
    "s_add_u32 s34, s26, s24\n"
    "s_add_u32 s26, s26, s22\n"
    "s_mul_i32 s18, s26, 36864\n"
    "s_lshl_b32 s26, s26, 17\n"
    "s_lshl_b32 s27, s31, 11\n"
    "s_add_u32 s26, s26, s27\n"
    "v_lshlrev_b32 v6, 12, v3\n"
    "v_add_u32 v6, s26, v6\n"
    "v_add_u32 v19, v6, v5\n"
    "v_add_u32 v20, v6, v8\n"
    "v_add_u32 v21, 63488, v19\n"
    "v_add_u32 v22, 95232, v20\n"
    "v_add_u32 v20, 31744, v20\n"
    "s_lshl_b32 s26, s35, 1\n"
    "s_lshr_b32 s27, s24, 1\n"
    "s_lshl_b32 s27, s27, 3\n"
    "s_add_u32 s26, s26, s27\n"
    "s_and_b32 s26, s26, 15\n"
    "s_lshl_b32 s62, s26, 7\n"
    "v_mov_b32 v45, 0\n"
    "s_lshl_b32 s19, s24, 8\n"
    "s_add_u32 s19, s19, 15335424\n"
    "s_add_u32 s60, s60, 512\n"
    "s_add_u32 s61, s61, 512\n"
    "s_add_u32 m0, s59, 65536\n"
    "s_nop 0\n"
    "buffer_load_dwordx4 v10, s[36:39], s60 offen offset:0 lds\n"
    "buffer_load_dwordx4 v11, s[36:39], s60 offen offset:1024 lds\n"
    "buffer_load_dwordx4 v12, s[36:39], s60 offen offset:2048 lds\n"
    "buffer_load_dwordx4 v13, s[36:39], s60 offen offset:3072 lds\n"
    "s_add_u32 m0, s59, 0\n"
    "s_nop 0\n"
    "buffer_load_dwordx4 v14, s[40:43], s61 offen offset:0 lds\n"
    "buffer_load_dwordx4 v15, s[40:43], s61 offen offset:1024 lds\n"
    "buffer_load_dwordx4 v16, s[40:43], s61 offen offset:2048 lds\n"
    "buffer_load_dwordx4 v17, s[40:43], s61 offen offset:3072 lds\n"
    "buffer_load_dwordx2 v[38:39], v45, s[48:51], s19 offen sc1\n"
    "s_lshl_b32 s69, s26, 1\n"
    "s_add_u32 s69, s69, s22\n"
    "s_add_u32 s56, s69, s22\n"
    "v_lshlrev_b32 v23, 4, v1\n"
    "s_lshl_b32 s26, s25, 2\n"
    "s_add_u32 s26, s26, s24\n"
    "s_mul_i32 s68, s26, 294912\n"
    "s_lshl_b32 s35, s26, 12\n"
    "s_add_u32 s35, s35, 15204352\n"
    "s_lshl_b32 s20, s23, 8\n"
    "s_add_u32 s20, s20, s35\n"
    "v_lshlrev_b32 v42, 8, v2\n"
    "v_add_u32 v42, s35, v42\n"
    "v_mov_b32 v9, s19\n"
    "s_cmp_eq_u32 s22, 0\n"
    "s_cselect_b64 vcc, -1, 0\n"
    "v_cndmask_b32 v42, v9, v42, vcc\n"
    "v_mov_b32 v30, v23\n"
    "v_add_u32 v31, 0x10000, v23\n"
    "v_add_u32 v32, 0x20000, v23\n"
    "v_and_b32 v9, 3, v1\n"
    "v_min_u32 v9, 2, v9\n"
    "s_add_u32 s26, s25, 1\n"
    "v_add_u32 v9, s26, v9\n"
    "v_and_b32 v9, 3, v9\n"
    "v_lshlrev_b32 v44, 8, v9\n"
    "v_mov_b32 v60, -1\n"
    "v_mov_b32 v61, -1\n"
    "v_mov_b32 v62, -1\n"
    "v_mov_b32 v63, -1\n"
    "s_lshl_b32 s7, s34, 2\n"
    "s_add_u32 s26, s25, 1\n"
    "s_and_b32 s26, s26, 3\n"
    "s_add_u32 s26, s26, s7\n"
    "s_lshl_b32 s26, s26, 2\n"
    "s_add_u32 s26, s26, s22\n"
    "s_mul_i32 s26, s26, 9216\n"
    "s_add_u32 s11, s26, 4718592\n"
    "s_add_u32 s26, s25, 2\n"
    "s_and_b32 s26, s26, 3\n"
    "s_add_u32 s26, s26, s7\n"
    "s_lshl_b32 s26, s26, 2\n"
    "s_add_u32 s26, s26, s22\n"
    "s_mul_i32 s26, s26, 9216\n"
    "s_add_u32 s12, s26, 4721664\n"
    "s_add_u32 s26, s25, 3\n"
    "s_and_b32 s26, s26, 3\n"
    "s_add_u32 s26, s26, s7\n"
    "s_lshl_b32 s26, s26, 2\n"
    "s_add_u32 s26, s26, s22\n"
    "s_mul_i32 s26, s26, 9216\n"
    "s_add_u32 s13, s26, 4724736\n"
    "s_add_u32 s26, s7, s25\n"
    "s_lshl_b32 s26, s26, 2\n"
    "s_add_u32 s26, s26, s22\n"
    "s_mul_i32 s26, s26, 9216\n"
    "s_add_u32 s15, s26, 4718592\n"
    "s_sub_u32 s26, s22, s25\n"
    "s_add_u32 s26, s26, 3\n"
    "s_and_b32 s26, s26, 3\n"
    "s_mul_i32 s26, s26, 3072\n"
    "s_lshl_b32 s29, s7, 2\n"
    "s_mul_i32 s30, s22, 5\n"
    "s_add_u32 s29, s29, s30\n"
    "s_mul_i32 s29, s29, 9216\n"
    "s_add_u32 s26, s26, s29\n"
    "s_add_u32 s26, s26, 4720640\n"
    "s_cmp_eq_u32 s22, s25\n"
    "s_cselect_b32 s30, 1, 0\n"
    "s_and_b32 s30, s30, s22\n"
    "s_mul_i32 s30, s30, 18432\n"
    "s_sub_u32 s14, s26, s30\n"
    "s_add_u32 s26, s7, s22\n"
    "s_lshl_b32 s26, s26, 11\n"
    "s_add_u32 s21, s26, 14680064\n"
    "s_lshl_b32 s29, s24, 2\n"
    "s_add_u32 s29, s29, s25\n"
    "s_lshl_b32 s29, s29, 6\n"
    "s_add_u32 s16, s18, s29\n"
    "s_add_u32 s17, s16, 18432\n"
    "s_lshl_b32 s29, s24, 5\n"
    "s_add_u32 s29, s29, 768\n"
    "s_add_u32 s18, s18, s29\n"
    "v_mul_u32_u24 v9, 1152, v2\n"
    "v_lshl_add_u32 v46, v4, 4, v9\n"
    "v_lshl_add_u32 v47, v4, 3, v9\n"
    "v_and_b32 v9, 1, v2\n"
    "v_lshlrev_b32 v9, 4, v9\n"
    "v_mov_b32 v8, 0x3f80\n"
    "v_lshlrev_b32 v8, v9, v8\n"
    "v_lshrrev_b32 v9, 2, v2\n"
    "v_cmp_eq_u32 vcc, v9, v4\n"
    "s_nop 1\n"
    "v_cndmask_b32 v8, 0, v8, vcc\n"
    "v_and_b32 v9, 2, v2\n"
    "v_cmp_eq_u32 vcc, 0, v9\n"
    "v_mov_b32 v50, 0\n"
    "v_mov_b32 v51, 0\n"
    "v_cndmask_b32 v48, 0, v8, vcc\n"
    "v_sub_u32 v49, v8, v48\n"
    "v_mov_b32 v52, 0\n"
    "v_mov_b32 v53, 0\n"
    "v_mov_b32 v54, v48\n"
    "v_mov_b32 v55, v49\n"
    "s_add_u32 s26, s25, 1\n"
    "s_xor_b32 s26, s26, s25\n"
    "s_bitcmp1_b32 s26, 1\n"
    "s_cselect_b32 s26, 0x80008000, 0\n"
    "v_xor_b32 v192, s26, v48\n"
    "v_xor_b32 v193, s26, v49\n"
    "v_mov_b32 v194, 0\n"
    "v_mov_b32 v195, 0\n"
    "v_mov_b32 v196, 0\n"
    "v_mov_b32 v197, 0\n"
    "v_xor_b32 v198, s26, v54\n"
    "v_xor_b32 v199, s26, v55\n"
    "s_add_u32 s26, s25, 2\n"
    "s_xor_b32 s26, s26, s25\n"
    "s_bitcmp1_b32 s26, 1\n"
    "s_cselect_b32 s26, 0x80008000, 0\n"
    "v_xor_b32 v184, s26, v48\n"
    "v_xor_b32 v185, s26, v49\n"
    "v_mov_b32 v186, 0\n"
    "v_mov_b32 v187, 0\n"
    "v_mov_b32 v188, 0\n"
    "v_mov_b32 v189, 0\n"
    "v_xor_b32 v190, s26, v54\n"
    "v_xor_b32 v191, s26, v55\n"
    "s_add_u32 s26, s25, 3\n"
    "s_xor_b32 s26, s26, s25\n"
    "s_bitcmp1_b32 s26, 1\n"
    "s_cselect_b32 s26, 0x80008000, 0\n"
    "v_xor_b32 v48, s26, v48\n"
    "v_xor_b32 v49, s26, v49\n"
    "v_xor_b32 v54, s26, v54\n"
    "v_xor_b32 v55, s26, v55\n"
    "s_add_u32 s26, s15, 3072\n"
    "s_add_u32 s27, s15, 6144\n"
    "s_lshl_b32 s29, s25, 8\n"
    "s_add_u32 s29, s29, s21\n"
    "s_cmp_eq_u32 s22, s25\n"
    "s_cselect_b64 s[30:31], -1, 1\n"
    "s_add_u32 m0, s59, 81920\n"
    "s_add_u32 s63, s62, 0\n"
    "s_and_b32 s63, s63, 2047\n"
    "buffer_load_dwordx4 v19, s[44:47], s63 offen offset:0 lds\n"
    "buffer_load_dwordx4 v20, s[44:47], s63 offen offset:1024 lds\n"
    "buffer_load_dwordx4 v21, s[44:47], s63 offen offset:2048 lds\n"
    "buffer_load_dwordx4 v22, s[44:47], s63 offen offset:3072 lds\n"
    "s_waitcnt vmcnt(4)\n"
    "v_add_co_u32 v38, vcc, 1, v38\n"
    "s_nop 1\n"
    "v_addc_co_u32 v39, vcc, 0, v39, vcc\n"
    "s_barrier\n"
    "s_cmp_eq_u32 s22, 0\n"
    "s_cbranch_scc0 L11_%=\n"
    "s_mov_b64 exec, 1\n"
    "buffer_store_dwordx2 v[38:39], v45, s[48:51], s20 offen sc1\n"
    "s_mov_b64 exec, -1\n"
    "L11_%=:\n"
    "s_add_u32 m0, s59, 98304\n"
    "s_add_u32 s63, s62, 128\n"
    "s_and_b32 s63, s63, 2047\n"
    "buffer_load_dwordx4 v19, s[44:47], s63 offen offset:0 lds\n"
    "buffer_load_dwordx4 v20, s[44:47], s63 offen offset:1024 lds\n"
    "buffer_load_dwordx4 v21, s[44:47], s63 offen offset:2048 lds\n"
    "buffer_load_dwordx4 v22, s[44:47], s63 offen offset:3072 lds\n"
    "s_add_u32 m0, s59, 114688\n"
    "s_add_u32 s63, s62, 256\n"
    "s_and_b32 s63, s63, 2047\n"
    "buffer_load_dwordx4 v19, s[44:47], s63 offen offset:0 lds\n"
    "buffer_load_dwordx4 v20, s[44:47], s63 offen offset:1024 lds\n"
    "buffer_load_dwordx4 v21, s[44:47], s63 offen offset:2048 lds\n"
    "buffer_load_dwordx4 v22, s[44:47], s63 offen offset:3072 lds\n"
    "s_waitcnt vmcnt(8)\n"
    "ds_read_b128 a[0:3], v28 offset:16384\n"
    "ds_read_b128 a[4:7], v28 offset:18432\n"
    "ds_read_b128 a[8:11], v29 offset:16384\n"
    "ds_read_b128 a[12:15], v29 offset:18432\n"
    "s_add_u32 m0, s59, 65536\n"
    "s_add_u32 s63, s62, 384\n"
    "s_and_b32 s63, s63, 2047\n"
    "buffer_load_dwordx4 v19, s[44:47], s63 offen offset:0 lds\n"
    "buffer_load_dwordx4 v20, s[44:47], s63 offen offset:1024 lds\n"
    "buffer_load_dwordx4 v21, s[44:47], s63 offen offset:2048 lds\n"
    "buffer_load_dwordx4 v22, s[44:47], s63 offen offset:3072 lds\n"
    "s_waitcnt vmcnt(8)\n"
    "ds_read_b128 a[16:19], v28 offset:32768\n"
    "ds_read_b128 a[20:23], v28 offset:34816\n"
    "ds_read_b128 a[24:27], v29 offset:32768\n"
    "ds_read_b128 a[28:31], v29 offset:34816\n"
    "s_waitcnt lgkmcnt(0)\n"
    "s_add_u32 m0, s59, 81920\n"
    "s_add_u32 s63, s62, 512\n"
    "s_and_b32 s63, s63, 2047\n"
    "buffer_load_dwordx4 v19, s[44:47], s63 offen offset:0 lds\n"
    "buffer_load_dwordx4 v20, s[44:47], s63 offen offset:1024 lds\n"
    "buffer_load_dwordx4 v21, s[44:47], s63 offen offset:2048 lds\n"
    "buffer_load_dwordx4 v22, s[44:47], s63 offen offset:3072 lds\n"
    "s_waitcnt vmcnt(8)\n"
    "ds_read_b128 a[32:35], v28 offset:49152\n"
    "ds_read_b128 a[36:39], v28 offset:51200\n"
    "ds_read_b128 a[40:43], v29 offset:49152\n"
    "ds_read_b128 a[44:47], v29 offset:51200\n"
    "s_add_u32 m0, s59, 98304\n"
    "s_add_u32 s63, s62, 640\n"
    "s_and_b32 s63, s63, 2047\n"
    "buffer_load_dwordx4 v19, s[44:47], s63 offen offset:0 lds\n"
    "buffer_load_dwordx4 v20, s[44:47], s63 offen offset:1024 lds\n"
    "buffer_load_dwordx4 v21, s[44:47], s63 offen offset:2048 lds\n"
    "buffer_load_dwordx4 v22, s[44:47], s63 offen offset:3072 lds\n"
    "s_waitcnt vmcnt(8)\n"
    "ds_read_b128 a[48:51], v28 offset:0\n"
    "ds_read_b128 a[52:55], v28 offset:2048\n"
    "ds_read_b128 a[56:59], v29 offset:0\n"
    "ds_read_b128 a[60:63], v29 offset:2048\n"
    "s_waitcnt lgkmcnt(0)\n"
    "s_add_u32 m0, s59, 114688\n"
    "s_add_u32 s63, s62, 768\n"
    "s_and_b32 s63, s63, 2047\n"
    "buffer_load_dwordx4 v19, s[44:47], s63 offen offset:0 lds\n"
    "buffer_load_dwordx4 v20, s[44:47], s63 offen offset:1024 lds\n"
    "buffer_load_dwordx4 v21, s[44:47], s63 offen offset:2048 lds\n"
    "buffer_load_dwordx4 v22, s[44:47], s63 offen offset:3072 lds\n"
    "s_waitcnt vmcnt(8)\n"
    "ds_read_b128 a[64:67], v28 offset:16384\n"
    "ds_read_b128 a[68:71], v28 offset:18432\n"
    "ds_read_b128 a[72:75], v29 offset:16384\n"
    "ds_read_b128 a[76:79], v29 offset:18432\n"
    "s_add_u32 m0, s59, 65536\n"
    "s_add_u32 s63, s62, 896\n"
    "s_and_b32 s63, s63, 2047\n"
    "buffer_load_dwordx4 v19, s[44:47], s63 offen offset:0 lds\n"
    "buffer_load_dwordx4 v20, s[44:47], s63 offen offset:1024 lds\n"
    "buffer_load_dwordx4 v21, s[44:47], s63 offen offset:2048 lds\n"
    "buffer_load_dwordx4 v22, s[44:47], s63 offen offset:3072 lds\n"
    "s_waitcnt vmcnt(8)\n"
    "ds_read_b128 a[80:83], v28 offset:32768\n"
    "ds_read_b128 a[84:87], v28 offset:34816\n"
    "ds_read_b128 a[88:91], v29 offset:32768\n"
    "ds_read_b128 a[92:95], v29 offset:34816\n"
    "s_waitcnt lgkmcnt(0)\n"
    "s_add_u32 m0, s59, 81920\n"
    "s_add_u32 s63, s62, 1024\n"
    "s_and_b32 s63, s63, 2047\n"
    "buffer_load_dwordx4 v19, s[44:47], s63 offen offset:0 lds\n"
    "buffer_load_dwordx4 v20, s[44:47], s63 offen offset:1024 lds\n"
    "buffer_load_dwordx4 v21, s[44:47], s63 offen offset:2048 lds\n"
    "buffer_load_dwordx4 v22, s[44:47], s63 offen offset:3072 lds\n"
    "s_waitcnt vmcnt(8)\n"
    "ds_read_b128 a[96:99], v28 offset:49152\n"
    "ds_read_b128 a[100:103], v28 offset:51200\n"
    "ds_read_b128 a[104:107], v29 offset:49152\n"
    "ds_read_b128 a[108:111], v29 offset:51200\n"
    "s_add_u32 m0, s59, 98304\n"
    "s_add_u32 s63, s62, 1152\n"
    "s_and_b32 s63, s63, 2047\n"
    "buffer_load_dwordx4 v19, s[44:47], s63 offen offset:0 lds\n"
    "buffer_load_dwordx4 v20, s[44:47], s63 offen offset:1024 lds\n"
    "buffer_load_dwordx4 v21, s[44:47], s63 offen offset:2048 lds\n"
    "buffer_load_dwordx4 v22, s[44:47], s63 offen offset:3072 lds\n"
    "s_waitcnt vmcnt(8)\n"
    "ds_read_b128 a[112:115], v28 offset:0\n"
    "ds_read_b128 a[116:119], v28 offset:2048\n"
    "ds_read_b128 a[120:123], v29 offset:0\n"
    "ds_read_b128 a[124:127], v29 offset:2048\n"
    "buffer_load_dwordx2 v[40:41], v42, s[48:51], 0 offen offset:0 sc1\n"
    "s_waitcnt lgkmcnt(0)\n"
    "s_add_u32 m0, s59, 114688\n"
    "s_add_u32 s63, s62, 1280\n"
    "s_and_b32 s63, s63, 2047\n"
    "buffer_load_dwordx4 v19, s[44:47], s63 offen offset:0 lds\n"
    "buffer_load_dwordx4 v20, s[44:47], s63 offen offset:1024 lds\n"
    "buffer_load_dwordx4 v21, s[44:47], s63 offen offset:2048 lds\n"
    "buffer_load_dwordx4 v22, s[44:47], s63 offen offset:3072 lds\n"
    "s_waitcnt vmcnt(9)\n"
    "ds_read_b128 a[128:131], v28 offset:16384\n"
    "ds_read_b128 a[132:135], v28 offset:18432\n"
    "ds_read_b128 a[136:139], v29 offset:16384\n"
    "ds_read_b128 a[140:143], v29 offset:18432\n"
    "s_add_u32 m0, s59, 65536\n"
    "s_add_u32 s63, s62, 1408\n"
    "s_and_b32 s63, s63, 2047\n"
    "buffer_load_dwordx4 v19, s[44:47], s63 offen offset:0 lds\n"
    "buffer_load_dwordx4 v20, s[44:47], s63 offen offset:1024 lds\n"
    "buffer_load_dwordx4 v21, s[44:47], s63 offen offset:2048 lds\n"
    "buffer_load_dwordx4 v22, s[44:47], s63 offen offset:3072 lds\n"
    "s_waitcnt vmcnt(9)\n"
    "ds_read_b128 a[144:147], v28 offset:32768\n"
    "ds_read_b128 a[148:151], v28 offset:34816\n"
    "ds_read_b128 a[152:155], v29 offset:32768\n"
    "ds_read_b128 a[156:159], v29 offset:34816\n"
    "s_waitcnt vmcnt(8)\n"
    "s_cmp_eq_u32 s22, 0\n"
    "s_cbranch_scc0 L12_%=\n"
    "v_cmp_ne_u64 vcc, v[40:41], v[38:39]\n"
    "s_cbranch_vccnz L13_%=\n"
    "L12_%=:\n"
    "s_barrier\n"
    "s_add_u32 m0, s59, 131072\n"
    "s_add_u32 s67, s69, 0\n"
    "s_and_b32 s67, s67, 31\n"
    "s_mul_i32 s66, s67, 4096\n"
    "s_add_u32 s66, s66, s68\n"
    "buffer_load_dwordx4 v23, s[48:51], s66 offen offset:0 sc1 lds\n"
    "buffer_load_dwordx4 v23, s[48:51], s66 offen offset:1024 sc1 lds\n"
    "buffer_load_dwordx4 v23, s[48:51], s66 offen offset:2048 sc1 lds\n"
    "buffer_load_dwordx4 v23, s[48:51], s66 offen offset:3072 sc1 lds\n"
    "s_add_u32 m0, s59, 147456\n"
    "s_add_u32 s67, s69, 0\n"
    "s_and_b32 s67, s67, 31\n"
    "s_mul_i32 s66, s67, 3072\n"
    "s_add_u32 s66, s66, s68\n"
    "s_add_u32 s66, s66, 131072\n"
    "buffer_load_dwordx4 v23, s[48:51], s66 offen offset:0 sc1 lds\n"
    "buffer_load_dwordx4 v23, s[48:51], s66 offen offset:1024 sc1 lds\n"
    "buffer_load_dwordx4 v23, s[48:51], s66 offen offset:2048 sc1 lds\n"
    "s_add_u32 m0, s59, 0\n"
    "s_add_u32 s67, s69, 4\n"
    "s_and_b32 s67, s67, 31\n"
    "s_mul_i32 s66, s67, 4096\n"
    "s_add_u32 s66, s66, s68\n"
    "buffer_load_dwordx4 v23, s[48:51], s66 offen offset:0 sc1 lds\n"
    "buffer_load_dwordx4 v23, s[48:51], s66 offen offset:1024 sc1 lds\n"
    "buffer_load_dwordx4 v23, s[48:51], s66 offen offset:2048 sc1 lds\n"
    "buffer_load_dwordx4 v23, s[48:51], s66 offen offset:3072 sc1 lds\n"
    "s_waitcnt vmcnt(7)\n"
    "s_barrier\n"
    "ds_read_b128 v[136:139], v32 offset:0\n"
    "ds_read_b128 v[140:143], v32 offset:1024\n"
    "ds_read_b128 v[144:147], v32 offset:2048\n"
    "ds_read_b128 v[148:151], v32 offset:3072\n"
    "ds_read_b128 v[152:155], v32 offset:4096\n"
    "ds_read_b128 v[156:159], v32 offset:5120\n"
    "ds_read_b128 v[160:163], v32 offset:6144\n"
    "ds_read_b128 v[164:167], v32 offset:7168\n"
    "s_waitcnt lgkmcnt(5)\n"
    "v_mfma_f32_16x16x32_bf16 v[64:67], v[136:139], a[0:3], 0\n"
    "s_add_u32 m0, s59, 16384\n"
    "s_add_u32 s67, s69, 4\n"
    "s_and_b32 s67, s67, 31\n"
    "v_mfma_f32_16x16x32_bf16 v[68:71], v[136:139], a[4:7], 0\n"
    "ds_read_b128 v[168:171], v32 offset:8192\n"
    "s_mul_i32 s66, s67, 3072\n"
    "s_add_u32 s66, s66, s68\n"
    "v_mfma_f32_16x16x32_bf16 v[72:75], v[140:143], a[0:3], 0\n"
    "s_add_u32 s66, s66, 131072\n"
    "v_mfma_f32_16x16x32_bf16 v[76:79], v[140:143], a[4:7], 0\n"
    "ds_read_b128 v[172:175], v32 offset:9216\n"
    "v_mfma_f32_16x16x32_bf16 v[80:83], v[144:147], a[0:3], 0\n"
    "buffer_load_dwordx4 v23, s[48:51], s66 offen offset:0 sc1 lds\n"
    "v_mfma_f32_16x16x32_bf16 v[84:87], v[144:147], a[4:7], 0\n"
    "ds_read_b128 v[176:179], v32 offset:10240\n"
    "s_waitcnt lgkmcnt(5)\n"
    "v_mfma_f32_16x16x32_bf16 v[88:91], v[148:151], a[0:3], 0\n"
    "buffer_load_dwordx4 v23, s[48:51], s66 offen offset:1024 sc1 lds\n"
    "v_mfma_f32_16x16x32_bf16 v[92:95], v[148:151], a[4:7], 0\n"
    "ds_read_b128 v[180:183], v32 offset:11264\n"
    "v_mfma_f32_16x16x32_bf16 v[64:67], v[152:155], a[8:11], v[64:67]\n"
    "buffer_load_dwordx4 v23, s[48:51], s66 offen offset:2048 sc1 lds\n"
    "v_mfma_f32_16x16x32_bf16 v[68:71], v[152:155], a[12:15], v[68:71]\n"
    "ds_read_b128 v[136:139], v32 offset:12288\n"
    "s_add_u32 m0, s59, 32768\n"
    "s_add_u32 s67, s69, 8\n"
    "v_mfma_f32_16x16x32_bf16 v[72:75], v[156:159], a[8:11], v[72:75]\n"
    "ds_read_b128 a[160:163], v28 offset:49152\n"
    "ds_read_b128 a[164:167], v28 offset:51200\n"
    "ds_read_b128 a[168:171], v29 offset:49152\n"
    "v_mfma_f32_16x16x32_bf16 v[76:79], v[156:159], a[12:15], v[76:79]\n"
    "ds_read_b128 v[140:143], v32 offset:13312\n"
    "ds_read_b128 a[172:175], v29 offset:51200\n"
    "s_waitcnt lgkmcnt(9)\n"
    "v_mfma_f32_16x16x32_bf16 v[80:83], v[160:163], a[8:11], v[80:83]\n"
    "s_and_b32 s67, s67, 31\n"
    "s_mul_i32 s66, s67, 4096\n"
    "s_add_u32 s66, s66, s68\n"
    "v_mfma_f32_16x16x32_bf16 v[84:87], v[160:163], a[12:15], v[84:87]\n"
    "ds_read_b128 v[144:147], v32 offset:14336\n"
    "v_mfma_f32_16x16x32_bf16 v[88:91], v[164:167], a[8:11], v[88:91]\n"
    "buffer_load_dwordx4 v23, s[48:51], s66 offen offset:0 sc1 lds\n"
    "v_mfma_f32_16x16x32_bf16 v[92:95], v[164:167], a[12:15], v[92:95]\n"
    "ds_read_b128 v[148:151], v32 offset:15360\n"
    "v_mfma_f32_16x16x32_bf16 v[64:67], v[168:171], a[16:19], v[64:67]\n"
    "v_mfma_f32_16x16x32_bf16 v[68:71], v[168:171], a[20:23], v[68:71]\n"
    "s_waitcnt vmcnt(8)\n"
    "s_barrier\n"
    "ds_read_b128 v[152:155], v32 offset:16384\n"
    "s_waitcnt lgkmcnt(9)\n"
    "v_mfma_f32_16x16x32_bf16 v[72:75], v[172:175], a[16:19], v[72:75]\n"
    "buffer_load_dwordx4 v23, s[48:51], s66 offen offset:1024 sc1 lds\n"
    "v_mfma_f32_16x16x32_bf16 v[76:79], v[172:175], a[20:23], v[76:79]\n"
    "ds_read_b128 v[156:159], v32 offset:17408\n"
    "v_mfma_f32_16x16x32_bf16 v[80:83], v[176:179], a[16:19], v[80:83]\n"
    "v_mfma_f32_16x16x32_bf16 v[84:87], v[176:179], a[20:23], v[84:87]\n"
    "ds_read_b128 v[160:163], v32 offset:18432\n"
    "v_mfma_f32_16x16x32_bf16 v[88:91], v[180:183], a[16:19], v[88:91]\n"
    "v_mfma_f32_16x16x32_bf16 v[92:95], v[180:183], a[20:23], v[92:95]\n"
    "ds_read_b128 v[164:167], v32 offset:20480\n"
    "s_waitcnt lgkmcnt(5)\n"
    "v_mfma_f32_16x16x32_bf16 v[64:67], v[136:139], a[24:27], v[64:67]\n"
    "v_mfma_f32_16x16x32_bf16 v[68:71], v[136:139], a[28:31], v[68:71]\n"
    "ds_read_b128 v[168:171], v32 offset:21504\n"
    "v_mfma_f32_16x16x32_bf16 v[72:75], v[140:143], a[24:27], v[72:75]\n"
    "v_mfma_f32_16x16x32_bf16 v[76:79], v[140:143], a[28:31], v[76:79]\n"
    "ds_read_b128 v[172:175], v32 offset:22528\n"
    "v_mfma_f32_16x16x32_bf16 v[80:83], v[144:147], a[24:27], v[80:83]\n"
    "v_mfma_f32_16x16x32_bf16 v[84:87], v[144:147], a[28:31], v[84:87]\n"
    "ds_read_b128 v[176:179], v32 offset:24576\n"
    "s_waitcnt lgkmcnt(5)\n"
    "v_mfma_f32_16x16x32_bf16 v[88:91], v[148:151], a[24:27], v[88:91]\n"
    "v_mfma_f32_16x16x32_bf16 v[92:95], v[148:151], a[28:31], v[92:95]\n"
    "ds_read_b128 v[180:183], v32 offset:25600\n"
    "v_mfma_f32_16x16x32_bf16 v[96:99], v[152:155], a[0:3], 0\n"
    "v_mfma_f32_16x16x32_bf16 v[100:103], v[152:155], a[4:7], 0\n"
    "ds_read_b128 v[136:139], v32 offset:26624\n"
    "v_mfma_f32_16x16x32_bf16 v[104:107], v[156:159], a[0:3], 0\n"
    "v_mfma_f32_16x16x32_bf16 v[108:111], v[156:159], a[4:7], 0\n"
    "ds_read_b128 v[140:143], v32 offset:28672\n"
    "s_waitcnt lgkmcnt(5)\n"
    "v_mfma_f32_16x16x32_bf16 v[112:115], v[160:163], a[0:3], 0\n"
    "buffer_load_dwordx4 v23, s[48:51], s66 offen offset:2048 sc1 lds\n"
    "v_mfma_f32_16x16x32_bf16 v[116:119], v[160:163], a[4:7], 0\n"
    "ds_read_b128 v[144:147], v32 offset:29696\n"
    "v_mfma_f32_16x16x32_bf16 v[96:99], v[164:167], a[8:11], v[96:99]\n"
    "v_mfma_f32_16x16x32_bf16 v[100:103], v[164:167], a[12:15], v[100:103]\n"
    "ds_read_b128 v[148:151], v32 offset:30720\n"
    "v_mfma_f32_16x16x32_bf16 v[104:107], v[168:171], a[8:11], v[104:107]\n"
    "buffer_load_dwordx4 v23, s[48:51], s66 offen offset:3072 sc1 lds\n"
    "v_mfma_f32_16x16x32_bf16 v[108:111], v[168:171], a[12:15], v[108:111]\n"
    "s_add_u32 m0, s59, 49152\n"
    "s_add_u32 s67, s69, 8\n"
    "s_and_b32 s67, s67, 31\n"
    "s_mul_i32 s66, s67, 3072\n"
    "s_add_u32 s66, s66, s68\n"
    "s_add_u32 s66, s66, 131072\n"
    "buffer_load_dwordx4 v23, s[48:51], s66 offen offset:0 sc1 lds\n"
    "buffer_load_dwordx4 v23, s[48:51], s66 offen offset:1024 sc1 lds\n"
    "buffer_load_dwordx4 v23, s[48:51], s66 offen offset:2048 sc1 lds\n"
    "s_waitcnt vmcnt(7)\n"
    "s_barrier\n"
    "ds_read_b128 v[152:155], v30 offset:0\n"
    "s_waitcnt lgkmcnt(5)\n"
    "v_mfma_f32_16x16x32_bf16 v[112:115], v[172:175], a[8:11], v[112:115]\n"
    "s_add_u32 m0, s59, 81920\n"
    "s_add_u32 s63, s62, 1536\n"
    "s_and_b32 s63, s63, 2047\n"
    "v_mfma_f32_16x16x32_bf16 v[116:119], v[172:175], a[12:15], v[116:119]\n"
    "ds_read_b128 v[156:159], v30 offset:1024\n"
    "v_mfma_f32_16x16x32_bf16 v[96:99], v[176:179], a[16:19], v[96:99]\n"
    "buffer_load_dwordx4 v19, s[44:47], s63 offen offset:0 lds\n"
    "v_mfma_f32_16x16x32_bf16 v[100:103], v[176:179], a[20:23], v[100:103]\n"
    "ds_read_b128 v[160:163], v30 offset:2048\n"
    "v_mfma_f32_16x16x32_bf16 v[104:107], v[180:183], a[16:19], v[104:107]\n"
    "buffer_load_dwordx4 v20, s[44:47], s63 offen offset:1024 lds\n"
    "v_mfma_f32_16x16x32_bf16 v[108:111], v[180:183], a[20:23], v[108:111]\n"
    "ds_read_b128 v[164:167], v30 offset:3072\n"
    "s_waitcnt lgkmcnt(5)\n"
    "v_mfma_f32_16x16x32_bf16 v[112:115], v[136:139], a[16:19], v[112:115]\n"
    "v_mfma_f32_16x16x32_bf16 v[116:119], v[136:139], a[20:23], v[116:119]\n"
    "ds_read_b128 v[168:171], v30 offset:4096\n"
    "v_mfma_f32_16x16x32_bf16 v[96:99], v[140:143], a[24:27], v[96:99]\n"
    "buffer_load_dwordx4 v21, s[44:47], s63 offen offset:2048 lds\n"
    "v_mfma_f32_16x16x32_bf16 v[100:103], v[140:143], a[28:31], v[100:103]\n"
    "ds_read_b128 v[172:175], v30 offset:5120\n"
    "v_mfma_f32_16x16x32_bf16 v[104:107], v[144:147], a[24:27], v[104:107]\n"
    "v_mfma_f32_16x16x32_bf16 v[108:111], v[144:147], a[28:31], v[108:111]\n"
    "ds_read_b128 v[176:179], v30 offset:6144\n"
    "s_waitcnt lgkmcnt(5)\n"
    "v_mfma_f32_16x16x32_bf16 v[112:115], v[148:151], a[24:27], v[112:115]\n"
    "buffer_load_dwordx4 v22, s[44:47], s63 offen offset:3072 lds\n"
    "v_mfma_f32_16x16x32_bf16 v[116:119], v[148:151], a[28:31], v[116:119]\n"
    "ds_read_b128 v[180:183], v30 offset:7168\n"
    "s_add_u32 m0, s59, 131072\n"
    "s_add_u32 s67, s69, 12\n"
    "v_mfma_f32_16x16x32_bf16 v[64:67], v[152:155], a[32:35], v[64:67]\n"
    "s_and_b32 s67, s67, 31\n"
    "s_mul_i32 s66, s67, 4096\n"
    "s_add_u32 s66, s66, s68\n"
    "v_mfma_f32_16x16x32_bf16 v[68:71], v[152:155], a[36:39], v[68:71]\n"
    "ds_read_b128 v[136:139], v30 offset:8192\n"
    "v_mfma_f32_16x16x32_bf16 v[72:75], v[156:159], a[32:35], v[72:75]\n"
    "buffer_load_dwordx4 v23, s[48:51], s66 offen offset:0 sc1 lds\n"
    "v_mfma_f32_16x16x32_bf16 v[76:79], v[156:159], a[36:39], v[76:79]\n"
    "ds_read_b128 v[140:143], v30 offset:9216\n"
    "s_waitcnt lgkmcnt(5)\n"
    "v_mfma_f32_16x16x32_bf16 v[80:83], v[160:163], a[32:35], v[80:83]\n"
    "ds_read_b128 a[176:179], v28 offset:0\n"
    "ds_read_b128 a[180:183], v28 offset:2048\n"
    "ds_read_b128 a[184:187], v29 offset:0\n"
    "v_mfma_f32_16x16x32_bf16 v[84:87], v[160:163], a[36:39], v[84:87]\n"
    "ds_read_b128 v[144:147], v30 offset:10240\n"
    "ds_read_b128 a[188:191], v29 offset:2048\n"
    "v_mfma_f32_16x16x32_bf16 v[88:91], v[164:167], a[32:35], v[88:91]\n"
    "v_mfma_f32_16x16x32_bf16 v[92:95], v[164:167], a[36:39], v[92:95]\n"
    "ds_read_b128 v[148:151], v30 offset:11264\n"
    "v_mfma_f32_16x16x32_bf16 v[64:67], v[168:171], a[40:43], v[64:67]\n"
    "buffer_load_dwordx4 v23, s[48:51], s66 offen offset:1024 sc1 lds\n"
    "v_mfma_f32_16x16x32_bf16 v[68:71], v[168:171], a[44:47], v[68:71]\n"
    "ds_read_b128 v[152:155], v30 offset:12288\n"
    "s_waitcnt lgkmcnt(9)\n"
    "v_mfma_f32_16x16x32_bf16 v[72:75], v[172:175], a[40:43], v[72:75]\n"
    "v_mfma_f32_16x16x32_bf16 v[76:79], v[172:175], a[44:47], v[76:79]\n"
    "ds_read_b128 v[156:159], v30 offset:13312\n"
    "v_mfma_f32_16x16x32_bf16 v[80:83], v[176:179], a[40:43], v[80:83]\n"
    "buffer_load_dwordx4 v23, s[48:51], s66 offen offset:2048 sc1 lds\n"
    "v_mfma_f32_16x16x32_bf16 v[84:87], v[176:179], a[44:47], v[84:87]\n"
    "ds_read_b128 v[160:163], v30 offset:14336\n"
    "v_mfma_f32_16x16x32_bf16 v[88:91], v[180:183], a[40:43], v[88:91]\n"
    "v_mfma_f32_16x16x32_bf16 v[92:95], v[180:183], a[44:47], v[92:95]\n"
    "ds_read_b128 v[164:167], v30 offset:15360\n"
    "s_waitcnt lgkmcnt(6)\n"
    "v_mfma_f32_16x16x32_bf16 v[64:67], v[136:139], a[48:51], v[64:67]\n"
    "buffer_load_dwordx4 v23, s[48:51], s66 offen offset:3072 sc1 lds\n"
    "v_mfma_f32_16x16x32_bf16 v[68:71], v[136:139], a[52:55], v[68:71]\n"
    "ds_read_b128 v[168:171], v30 offset:16384\n"
    "s_add_u32 m0, s59, 98304\n"
    "s_add_u32 s63, s62, 1664\n"
    "v_mfma_f32_16x16x32_bf16 v[72:75], v[140:143], a[48:51], v[72:75]\n"
    "s_and_b32 s63, s63, 2047\n"
    "v_mfma_f32_16x16x32_bf16 v[76:79], v[140:143], a[52:55], v[76:79]\n"
    "ds_read_b128 v[172:175], v30 offset:17408\n"
    "v_mfma_f32_16x16x32_bf16 v[80:83], v[144:147], a[48:51], v[80:83]\n"
    "buffer_load_dwordx4 v19, s[44:47], s63 offen offset:0 lds\n"
    "v_mfma_f32_16x16x32_bf16 v[84:87], v[144:147], a[52:55], v[84:87]\n"
    "ds_read_b128 v[176:179], v30 offset:18432\n"
    "s_waitcnt lgkmcnt(5)\n"
    "v_mfma_f32_16x16x32_bf16 v[88:91], v[148:151], a[48:51], v[88:91]\n"
    "v_mfma_f32_16x16x32_bf16 v[92:95], v[148:151], a[52:55], v[92:95]\n"
    "ds_read_b128 v[180:183], v30 offset:20480\n"
    "v_mfma_f32_16x16x32_bf16 v[64:67], v[152:155], a[56:59], v[64:67]\n"
    "v_mfma_f32_16x16x32_bf16 v[68:71], v[152:155], a[60:63], v[68:71]\n"
    "ds_read_b128 v[136:139], v30 offset:21504\n"
    "v_mfma_f32_16x16x32_bf16 v[72:75], v[156:159], a[56:59], v[72:75]\n"
    "buffer_load_dwordx4 v20, s[44:47], s63 offen offset:1024 lds\n"
    "v_mfma_f32_16x16x32_bf16 v[76:79], v[156:159], a[60:63], v[76:79]\n"
    "ds_read_b128 v[140:143], v30 offset:22528\n"
    "s_waitcnt lgkmcnt(5)\n"
    "v_mfma_f32_16x16x32_bf16 v[80:83], v[160:163], a[56:59], v[80:83]\n"
    "v_mfma_f32_16x16x32_bf16 v[84:87], v[160:163], a[60:63], v[84:87]\n"
    "ds_read_b128 v[144:147], v30 offset:24576\n"
    "v_mfma_f32_16x16x32_bf16 v[88:91], v[164:167], a[56:59], v[88:91]\n"
    "buffer_load_dwordx4 v21, s[44:47], s63 offen offset:2048 lds\n"
    "v_mfma_f32_16x16x32_bf16 v[92:95], v[164:167], a[60:63], v[92:95]\n"
    "ds_read_b128 v[148:151], v30 offset:25600\n"
    "v_mfma_f32_16x16x32_bf16 v[96:99], v[168:171], a[32:35], v[96:99]\n"
    "v_mfma_f32_16x16x32_bf16 v[100:103], v[168:171], a[36:39], v[100:103]\n"
    "ds_read_b128 v[152:155], v30 offset:26624\n"
    "s_waitcnt lgkmcnt(5)\n"
    "v_mfma_f32_16x16x32_bf16 v[104:107], v[172:175], a[32:35], v[104:107]\n"
    "buffer_load_dwordx4 v22, s[44:47], s63 offen offset:3072 lds\n"
    "v_mfma_f32_16x16x32_bf16 v[108:111], v[172:175], a[36:39], v[108:111]\n"
    "ds_read_b128 v[156:159], v30 offset:28672\n"
    "s_add_u32 m0, s59, 65536\n"
    "s_add_u32 s63, s62, 1792\n"
    "v_mfma_f32_16x16x32_bf16 v[112:115], v[176:179], a[32:35], v[112:115]\n"
    "s_and_b32 s63, s63, 2047\n"
    "v_mfma_f32_16x16x32_bf16 v[116:119], v[176:179], a[36:39], v[116:119]\n"
    "ds_read_b128 v[160:163], v30 offset:29696\n"
    "v_mfma_f32_16x16x32_bf16 v[96:99], v[180:183], a[40:43], v[96:99]\n"
    "buffer_load_dwordx4 v19, s[44:47], s63 offen offset:0 lds\n"
    "v_mfma_f32_16x16x32_bf16 v[100:103], v[180:183], a[44:47], v[100:103]\n"
    "ds_read_b128 v[164:167], v30 offset:30720\n"
    "s_waitcnt lgkmcnt(5)\n"
    "v_mfma_f32_16x16x32_bf16 v[104:107], v[136:139], a[40:43], v[104:107]\n"
    "v_mfma_f32_16x16x32_bf16 v[108:111], v[136:139], a[44:47], v[108:111]\n"
    "s_waitcnt vmcnt(13)\n"
    "s_barrier\n"
    "ds_read_b128 v[168:171], v30 offset:32768\n"
    "v_mfma_f32_16x16x32_bf16 v[112:115], v[140:143], a[40:43], v[112:115]\n"
    "buffer_load_dwordx4 v20, s[44:47], s63 offen offset:1024 lds\n"
    "v_mfma_f32_16x16x32_bf16 v[116:119], v[140:143], a[44:47], v[116:119]\n"
    "ds_read_b128 v[172:175], v30 offset:33792\n"
    "v_mfma_f32_16x16x32_bf16 v[96:99], v[144:147], a[48:51], v[96:99]\n"
    "buffer_load_dwordx4 v21, s[44:47], s63 offen offset:2048 lds\n"
    "v_mfma_f32_16x16x32_bf16 v[100:103], v[144:147], a[52:55], v[100:103]\n"
    "ds_read_b128 v[176:179], v30 offset:34816\n"
    "s_waitcnt lgkmcnt(5)\n"
    "v_mfma_f32_16x16x32_bf16 v[104:107], v[148:151], a[48:51], v[104:107]\n"
    "v_mfma_f32_16x16x32_bf16 v[108:111], v[148:151], a[52:55], v[108:111]\n"
    "ds_read_b128 v[180:183], v30 offset:35840\n"
    "v_mfma_f32_16x16x32_bf16 v[112:115], v[152:155], a[48:51], v[112:115]\n"
    "buffer_load_dwordx4 v22, s[44:47], s63 offen offset:3072 lds\n"
    "v_mfma_f32_16x16x32_bf16 v[116:119], v[152:155], a[52:55], v[116:119]\n"
    "ds_read_b128 v[136:139], v30 offset:36864\n"
    "s_add_u32 m0, s59, 147456\n"
    "s_add_u32 s67, s69, 16\n"
    "v_mfma_f32_16x16x32_bf16 v[96:99], v[156:159], a[56:59], v[96:99]\n"
    "s_and_b32 s67, s67, 31\n"
    "s_mul_i32 s66, s67, 4096\n"
    "s_add_u32 s66, s66, s68\n"
    "v_mfma_f32_16x16x32_bf16 v[100:103], v[156:159], a[60:63], v[100:103]\n"
    "ds_read_b128 v[140:143], v30 offset:37888\n"
    "s_waitcnt lgkmcnt(5)\n"
    "v_mfma_f32_16x16x32_bf16 v[104:107], v[160:163], a[56:59], v[104:107]\n"
    "buffer_load_dwordx4 v23, s[48:51], s66 offen offset:0 sc1 lds\n"
    "v_mfma_f32_16x16x32_bf16 v[108:111], v[160:163], a[60:63], v[108:111]\n"
    "ds_read_b128 v[144:147], v30 offset:38912\n"
    "v_mfma_f32_16x16x32_bf16 v[112:115], v[164:167], a[56:59], v[112:115]\n"
    "s_waitcnt vmcnt(13)\n"
    "ds_read_b128 a[192:195], v28 offset:16384\n"
    "ds_read_b128 a[196:199], v28 offset:18432\n"
    "ds_read_b128 a[200:203], v29 offset:16384\n"
    "v_mfma_f32_16x16x32_bf16 v[116:119], v[164:167], a[60:63], v[116:119]\n"
    "ds_read_b128 v[148:151], v30 offset:39936\n"
    "ds_read_b128 a[204:207], v29 offset:18432\n"
    "v_mfma_f32_16x16x32_bf16 v[64:67], v[168:171], a[64:67], v[64:67]\n"
    "buffer_load_dwordx4 v23, s[48:51], s66 offen offset:1024 sc1 lds\n"
    "v_mfma_f32_16x16x32_bf16 v[68:71], v[168:171], a[68:71], v[68:71]\n"
    "ds_read_b128 v[152:155], v30 offset:40960\n"
    "s_waitcnt lgkmcnt(9)\n"
    "v_mfma_f32_16x16x32_bf16 v[72:75], v[172:175], a[64:67], v[72:75]\n"
    "v_mfma_f32_16x16x32_bf16 v[76:79], v[172:175], a[68:71], v[76:79]\n"
    "ds_read_b128 v[156:159], v30 offset:41984\n"
    "v_mfma_f32_16x16x32_bf16 v[80:83], v[176:179], a[64:67], v[80:83]\n"
    "buffer_load_dwordx4 v23, s[48:51], s66 offen offset:2048 sc1 lds\n"
    "v_mfma_f32_16x16x32_bf16 v[84:87], v[176:179], a[68:71], v[84:87]\n"
    "ds_read_b128 v[160:163], v30 offset:43008\n"
    "v_mfma_f32_16x16x32_bf16 v[88:91], v[180:183], a[64:67], v[88:91]\n"
    "v_mfma_f32_16x16x32_bf16 v[92:95], v[180:183], a[68:71], v[92:95]\n"
    "ds_read_b128 v[164:167], v30 offset:44032\n"
    "s_waitcnt lgkmcnt(9)\n"
    "v_mfma_f32_16x16x32_bf16 v[64:67], v[136:139], a[72:75], v[64:67]\n"
    "buffer_load_dwordx4 v23, s[48:51], s66 offen offset:3072 sc1 lds\n"
    "v_mfma_f32_16x16x32_bf16 v[68:71], v[136:139], a[76:79], v[68:71]\n"
    "ds_read_b128 v[168:171], v30 offset:45056\n"
    "s_add_u32 m0, s59, 0\n"
    "s_add_u32 s67, s69, 20\n"
    "v_mfma_f32_16x16x32_bf16 v[72:75], v[140:143], a[72:75], v[72:75]\n"
    "s_and_b32 s67, s67, 31\n"
    "s_mul_i32 s66, s67, 4096\n"
    "s_add_u32 s66, s66, s68\n"
    "v_mfma_f32_16x16x32_bf16 v[76:79], v[140:143], a[76:79], v[76:79]\n"
    "ds_read_b128 v[172:175], v30 offset:46080\n"
    "v_mfma_f32_16x16x32_bf16 v[80:83], v[144:147], a[72:75], v[80:83]\n"
    "v_mfma_f32_16x16x32_bf16 v[84:87], v[144:147], a[76:79], v[84:87]\n"
    "ds_read_b128 v[176:179], v30 offset:47104\n"
    "s_waitcnt lgkmcnt(5)\n"
    "v_mfma_f32_16x16x32_bf16 v[88:91], v[148:151], a[72:75], v[88:91]\n"
    "buffer_load_dwordx4 v23, s[48:51], s66 offen offset:0 sc1 lds\n"
    "v_mfma_f32_16x16x32_bf16 v[92:95], v[148:151], a[76:79], v[92:95]\n"
    "ds_read_b128 v[180:183], v30 offset:48128\n"
    "v_mfma_f32_16x16x32_bf16 v[64:67], v[152:155], a[80:83], v[64:67]\n"
    "v_mfma_f32_16x16x32_bf16 v[68:71], v[152:155], a[84:87], v[68:71]\n"
    "ds_read_b128 v[136:139], v30 offset:49152\n"
    "v_mfma_f32_16x16x32_bf16 v[72:75], v[156:159], a[80:83], v[72:75]\n"
    "buffer_load_dwordx4 v23, s[48:51], s66 offen offset:1024 sc1 lds\n"
    "v_mfma_f32_16x16x32_bf16 v[76:79], v[156:159], a[84:87], v[76:79]\n"
    "ds_read_b128 v[140:143], v30 offset:50176\n"
    "s_waitcnt lgkmcnt(5)\n"
    "v_mfma_f32_16x16x32_bf16 v[80:83], v[160:163], a[80:83], v[80:83]\n"
    "v_mfma_f32_16x16x32_bf16 v[84:87], v[160:163], a[84:87], v[84:87]\n"
    "ds_read_b128 v[144:147], v30 offset:51200\n"
    "v_mfma_f32_16x16x32_bf16 v[88:91], v[164:167], a[80:83], v[88:91]\n"
    "buffer_load_dwordx4 v23, s[48:51], s66 offen offset:2048 sc1 lds\n"
    "v_mfma_f32_16x16x32_bf16 v[92:95], v[164:167], a[84:87], v[92:95]\n"
    "ds_read_b128 v[148:151], v30 offset:53248\n"
    "v_mfma_f32_16x16x32_bf16 v[64:67], v[168:171], a[88:91], v[64:67]\n"
    "v_mfma_f32_16x16x32_bf16 v[68:71], v[168:171], a[92:95], v[68:71]\n"
    "ds_read_b128 v[152:155], v30 offset:54272\n"
    "s_waitcnt lgkmcnt(5)\n"
    "v_mfma_f32_16x16x32_bf16 v[72:75], v[172:175], a[88:91], v[72:75]\n"
    "v_mfma_f32_16x16x32_bf16 v[76:79], v[172:175], a[92:95], v[76:79]\n"
    "ds_read_b128 v[156:159], v30 offset:55296\n"
    "v_mfma_f32_16x16x32_bf16 v[80:83], v[176:179], a[88:91], v[80:83]\n"
    "buffer_load_dwordx4 v23, s[48:51], s66 offen offset:3072 sc1 lds\n"
    "v_mfma_f32_16x16x32_bf16 v[84:87], v[176:179], a[92:95], v[84:87]\n"
    "ds_read_b128 v[160:163], v30 offset:57344\n"
    "s_add_u32 m0, s59, 81920\n"
    "s_add_u32 s63, s62, 1920\n"
    "v_mfma_f32_16x16x32_bf16 v[88:91], v[180:183], a[88:91], v[88:91]\n"
    "s_and_b32 s63, s63, 2047\n"
    "v_mfma_f32_16x16x32_bf16 v[92:95], v[180:183], a[92:95], v[92:95]\n"
    "ds_read_b128 v[164:167], v30 offset:58368\n"
    "s_waitcnt lgkmcnt(5)\n"
    "v_mfma_f32_16x16x32_bf16 v[96:99], v[136:139], a[64:67], v[96:99]\n"
    "buffer_load_dwordx4 v19, s[44:47], s63 offen offset:0 lds\n"
    "v_mfma_f32_16x16x32_bf16 v[100:103], v[136:139], a[68:71], v[100:103]\n"
    "ds_read_b128 v[168:171], v30 offset:59392\n"
    "v_mfma_f32_16x16x32_bf16 v[104:107], v[140:143], a[64:67], v[104:107]\n"
    "v_mfma_f32_16x16x32_bf16 v[108:111], v[140:143], a[68:71], v[108:111]\n"
    "ds_read_b128 v[172:175], v30 offset:61440\n"
    "v_mfma_f32_16x16x32_bf16 v[112:115], v[144:147], a[64:67], v[112:115]\n"
    "buffer_load_dwordx4 v20, s[44:47], s63 offen offset:1024 lds\n"
    "v_mfma_f32_16x16x32_bf16 v[116:119], v[144:147], a[68:71], v[116:119]\n"
    "ds_read_b128 v[176:179], v30 offset:62464\n"
    "s_waitcnt lgkmcnt(5)\n"
    "v_mfma_f32_16x16x32_bf16 v[96:99], v[148:151], a[72:75], v[96:99]\n"
    "v_mfma_f32_16x16x32_bf16 v[100:103], v[148:151], a[76:79], v[100:103]\n"
    "ds_read_b128 v[180:183], v30 offset:63488\n"
    "v_mfma_f32_16x16x32_bf16 v[104:107], v[152:155], a[72:75], v[104:107]\n"
    "buffer_load_dwordx4 v21, s[44:47], s63 offen offset:2048 lds\n"
    "v_mfma_f32_16x16x32_bf16 v[108:111], v[152:155], a[76:79], v[108:111]\n"
    "s_waitcnt vmcnt(7)\n"
    "s_barrier\n"
    "ds_read_b128 v[136:139], v32 offset:0\n"
    "v_mfma_f32_16x16x32_bf16 v[112:115], v[156:159], a[72:75], v[112:115]\n"
    "v_mfma_f32_16x16x32_bf16 v[116:119], v[156:159], a[76:79], v[116:119]\n"
    "ds_read_b128 v[140:143], v32 offset:1024\n"
    "s_waitcnt lgkmcnt(5)\n"
    "v_mfma_f32_16x16x32_bf16 v[96:99], v[160:163], a[80:83], v[96:99]\n"
    "buffer_load_dwordx4 v22, s[44:47], s63 offen offset:3072 lds\n"
    "v_mfma_f32_16x16x32_bf16 v[100:103], v[160:163], a[84:87], v[100:103]\n"
    "ds_read_b128 v[144:147], v32 offset:2048\n"
    "s_add_u32 m0, s59, 16384\n"
    "s_add_u32 s67, s69, 24\n"
    "v_mfma_f32_16x16x32_bf16 v[104:107], v[164:167], a[80:83], v[104:107]\n"
    "s_and_b32 s67, s67, 31\n"
    "s_mul_i32 s66, s67, 4096\n"
    "s_add_u32 s66, s66, s68\n"
    "v_mfma_f32_16x16x32_bf16 v[108:111], v[164:167], a[84:87], v[108:111]\n"
    "ds_read_b128 v[148:151], v32 offset:3072\n"
    "v_mfma_f32_16x16x32_bf16 v[112:115], v[168:171], a[80:83], v[112:115]\n"
    "ds_read_b128 a[208:211], v28 offset:32768\n"
    "ds_read_b128 a[212:215], v28 offset:34816\n"
    "ds_read_b128 a[216:219], v29 offset:32768\n"
    "v_mfma_f32_16x16x32_bf16 v[116:119], v[168:171], a[84:87], v[116:119]\n"
    "ds_read_b128 v[152:155], v32 offset:4096\n"
    "ds_read_b128 a[220:223], v29 offset:34816\n"
    "s_waitcnt lgkmcnt(9)\n"
    "v_mfma_f32_16x16x32_bf16 v[96:99], v[172:175], a[88:91], v[96:99]\n"
    "buffer_load_dwordx4 v23, s[48:51], s66 offen offset:0 sc1 lds\n"
    "v_mfma_f32_16x16x32_bf16 v[100:103], v[172:175], a[92:95], v[100:103]\n"
    "ds_read_b128 v[156:159], v32 offset:5120\n"
    "v_mfma_f32_16x16x32_bf16 v[104:107], v[176:179], a[88:91], v[104:107]\n"
    "v_mfma_f32_16x16x32_bf16 v[108:111], v[176:179], a[92:95], v[108:111]\n"
    "ds_read_b128 v[160:163], v32 offset:6144\n"
    "v_mfma_f32_16x16x32_bf16 v[112:115], v[180:183], a[88:91], v[112:115]\n"
    "buffer_load_dwordx4 v23, s[48:51], s66 offen offset:1024 sc1 lds\n"
    "v_mfma_f32_16x16x32_bf16 v[116:119], v[180:183], a[92:95], v[116:119]\n"
    "ds_read_b128 v[164:167], v32 offset:7168\n"
    "s_waitcnt lgkmcnt(9)\n"
    "v_mfma_f32_16x16x32_bf16 v[64:67], v[136:139], a[96:99], v[64:67]\n"
    "v_mfma_f32_16x16x32_bf16 v[68:71], v[136:139], a[100:103], v[68:71]\n"
    "ds_read_b128 v[168:171], v32 offset:8192\n"
    "v_mfma_f32_16x16x32_bf16 v[72:75], v[140:143], a[96:99], v[72:75]\n"
    "buffer_load_dwordx4 v23, s[48:51], s66 offen offset:2048 sc1 lds\n"
    "v_mfma_f32_16x16x32_bf16 v[76:79], v[140:143], a[100:103], v[76:79]\n"
    "ds_read_b128 v[172:175], v32 offset:9216\n"
    "v_mfma_f32_16x16x32_bf16 v[80:83], v[144:147], a[96:99], v[80:83]\n"
    "v_mfma_f32_16x16x32_bf16 v[84:87], v[144:147], a[100:103], v[84:87]\n"
    "ds_read_b128 v[176:179], v32 offset:10240\n"
    "s_waitcnt lgkmcnt(5)\n"
    "v_mfma_f32_16x16x32_bf16 v[88:91], v[148:151], a[96:99], v[88:91]\n"
    "buffer_load_dwordx4 v23, s[48:51], s66 offen offset:3072 sc1 lds\n"
    "v_mfma_f32_16x16x32_bf16 v[92:95], v[148:151], a[100:103], v[92:95]\n"
    "ds_read_b128 v[180:183], v32 offset:11264\n"
    "s_add_u32 m0, s59, 32768\n"
    "s_add_u32 s67, s69, 28\n"
    "v_mfma_f32_16x16x32_bf16 v[64:67], v[152:155], a[104:107], v[64:67]\n"
    "s_and_b32 s67, s67, 31\n"
    "s_mul_i32 s66, s67, 4096\n"
    "s_add_u32 s66, s66, s68\n"
    "v_mfma_f32_16x16x32_bf16 v[68:71], v[152:155], a[108:111], v[68:71]\n"
    "ds_read_b128 v[136:139], v32 offset:12288\n"
    "v_mfma_f32_16x16x32_bf16 v[72:75], v[156:159], a[104:107], v[72:75]\n"
    "v_mfma_f32_16x16x32_bf16 v[76:79], v[156:159], a[108:111], v[76:79]\n"
    "ds_read_b128 v[140:143], v32 offset:13312\n"
    "s_waitcnt lgkmcnt(5)\n"
    "v_mfma_f32_16x16x32_bf16 v[80:83], v[160:163], a[104:107], v[80:83]\n"
    "buffer_load_dwordx4 v23, s[48:51], s66 offen offset:0 sc1 lds\n"
    "v_mfma_f32_16x16x32_bf16 v[84:87], v[160:163], a[108:111], v[84:87]\n"
    "ds_read_b128 v[144:147], v32 offset:14336\n"
    "v_mfma_f32_16x16x32_bf16 v[88:91], v[164:167], a[104:107], v[88:91]\n"
    "v_mfma_f32_16x16x32_bf16 v[92:95], v[164:167], a[108:111], v[92:95]\n"
    "ds_read_b128 v[148:151], v32 offset:15360\n"
    "v_mfma_f32_16x16x32_bf16 v[64:67], v[168:171], a[112:115], v[64:67]\n"
    "buffer_load_dwordx4 v23, s[48:51], s66 offen offset:1024 sc1 lds\n"
    "v_mfma_f32_16x16x32_bf16 v[68:71], v[168:171], a[116:119], v[68:71]\n"
    "ds_read_b128 v[152:155], v32 offset:16384\n"
    "s_waitcnt lgkmcnt(5)\n"
    "v_mfma_f32_16x16x32_bf16 v[72:75], v[172:175], a[112:115], v[72:75]\n"
    "v_mfma_f32_16x16x32_bf16 v[76:79], v[172:175], a[116:119], v[76:79]\n"
    "ds_read_b128 v[156:159], v32 offset:17408\n"
    "v_mfma_f32_16x16x32_bf16 v[80:83], v[176:179], a[112:115], v[80:83]\n"
    "buffer_load_dwordx4 v23, s[48:51], s66 offen offset:2048 sc1 lds\n"
    "v_mfma_f32_16x16x32_bf16 v[84:87], v[176:179], a[116:119], v[84:87]\n"
    "ds_read_b128 v[160:163], v32 offset:18432\n"
    "v_mfma_f32_16x16x32_bf16 v[88:91], v[180:183], a[112:115], v[88:91]\n"
    "v_mfma_f32_16x16x32_bf16 v[92:95], v[180:183], a[116:119], v[92:95]\n"
    "ds_read_b128 v[164:167], v32 offset:19456\n"
    "s_waitcnt lgkmcnt(5)\n"
    "v_mfma_f32_16x16x32_bf16 v[64:67], v[136:139], a[120:123], v[64:67]\n"
    "buffer_load_dwordx4 v23, s[48:51], s66 offen offset:3072 sc1 lds\n"
    "v_mfma_f32_16x16x32_bf16 v[68:71], v[136:139], a[124:127], v[68:71]\n"
    "ds_read_b128 v[168:171], v32 offset:20480\n"
    "s_add_u32 m0, s59, 98304\n"
    "s_add_u32 s67, s69, 12\n"
    "v_mfma_f32_16x16x32_bf16 v[72:75], v[140:143], a[120:123], v[72:75]\n"
    "s_and_b32 s67, s67, 31\n"
    "s_mul_i32 s66, s67, 3072\n"
    "s_add_u32 s66, s66, s68\n"
    "v_mfma_f32_16x16x32_bf16 v[76:79], v[140:143], a[124:127], v[76:79]\n"
    "ds_read_b128 v[172:175], v32 offset:21504\n"
    "s_add_u32 s66, s66, 131072\n"
    "v_mfma_f32_16x16x32_bf16 v[80:83], v[144:147], a[120:123], v[80:83]\n"
    "v_mfma_f32_16x16x32_bf16 v[84:87], v[144:147], a[124:127], v[84:87]\n"
    "ds_read_b128 v[176:179], v32 offset:22528\n"
    "s_waitcnt lgkmcnt(5)\n"
    "v_mfma_f32_16x16x32_bf16 v[88:91], v[148:151], a[120:123], v[88:91]\n"
    "buffer_load_dwordx4 v23, s[48:51], s66 offen offset:0 sc1 lds\n"
    "v_mfma_f32_16x16x32_bf16 v[92:95], v[148:151], a[124:127], v[92:95]\n"
    "ds_read_b128 v[180:183], v32 offset:23552\n"
    "v_mfma_f32_16x16x32_bf16 v[64:67], v[152:155], a[128:131], v[64:67]\n"
    "v_mfma_f32_16x16x32_bf16 v[68:71], v[152:155], a[132:135], v[68:71]\n"
    "ds_read_b128 v[136:139], v32 offset:24576\n"
    "v_mfma_f32_16x16x32_bf16 v[72:75], v[156:159], a[128:131], v[72:75]\n"
    "buffer_load_dwordx4 v23, s[48:51], s66 offen offset:1024 sc1 lds\n"
    "v_mfma_f32_16x16x32_bf16 v[76:79], v[156:159], a[132:135], v[76:79]\n"
    "ds_read_b128 v[140:143], v32 offset:25600\n"
    "s_waitcnt lgkmcnt(5)\n"
    "v_mfma_f32_16x16x32_bf16 v[80:83], v[160:163], a[128:131], v[80:83]\n"
    "v_mfma_f32_16x16x32_bf16 v[84:87], v[160:163], a[132:135], v[84:87]\n"
    "ds_read_b128 v[144:147], v32 offset:26624\n"
    "v_mfma_f32_16x16x32_bf16 v[88:91], v[164:167], a[128:131], v[88:91]\n"
    "buffer_load_dwordx4 v23, s[48:51], s66 offen offset:2048 sc1 lds\n"
    "v_mfma_f32_16x16x32_bf16 v[92:95], v[164:167], a[132:135], v[92:95]\n"
    "ds_read_b128 v[148:151], v32 offset:27648\n"
    "s_add_u32 m0, s59, 114688\n"
    "s_add_u32 s67, s69, 16\n"
    "v_mfma_f32_16x16x32_bf16 v[64:67], v[168:171], a[136:139], v[64:67]\n"
    "s_and_b32 s67, s67, 31\n"
    "s_mul_i32 s66, s67, 3072\n"
    "s_add_u32 s66, s66, s68\n"
    "v_mfma_f32_16x16x32_bf16 v[68:71], v[168:171], a[140:143], v[68:71]\n"
    "ds_read_b128 v[152:155], v32 offset:28672\n"
    "s_add_u32 s66, s66, 131072\n"
    "s_waitcnt lgkmcnt(5)\n"
    "v_mfma_f32_16x16x32_bf16 v[72:75], v[172:175], a[136:139], v[72:75]\n"
    "ds_read_b128 a[224:227], v28 offset:0\n"
    "ds_read_b128 a[228:231], v28 offset:2048\n"
    "ds_read_b128 a[232:235], v29 offset:0\n"
    "v_mfma_f32_16x16x32_bf16 v[76:79], v[172:175], a[140:143], v[76:79]\n"
    "ds_read_b128 v[156:159], v32 offset:29696\n"
    "ds_read_b128 a[236:239], v29 offset:2048\n"
    "v_mfma_f32_16x16x32_bf16 v[80:83], v[176:179], a[136:139], v[80:83]\n"
    "buffer_load_dwordx4 v23, s[48:51], s66 offen offset:0 sc1 lds\n"
    "v_mfma_f32_16x16x32_bf16 v[84:87], v[176:179], a[140:143], v[84:87]\n"
    "ds_read_b128 v[160:163], v32 offset:30720\n"
    "v_mfma_f32_16x16x32_bf16 v[88:91], v[180:183], a[136:139], v[88:91]\n"
    "v_mfma_f32_16x16x32_bf16 v[92:95], v[180:183], a[140:143], v[92:95]\n"
    "ds_read_b128 v[164:167], v32 offset:31744\n"
    "s_waitcnt lgkmcnt(9)\n"
    "v_mfma_f32_16x16x32_bf16 v[64:67], v[136:139], a[144:147], v[64:67]\n"
    "buffer_load_dwordx4 v23, s[48:51], s66 offen offset:1024 sc1 lds\n"
    "v_mfma_f32_16x16x32_bf16 v[68:71], v[136:139], a[148:151], v[68:71]\n"
    "s_waitcnt vmcnt(9)\n"
    "s_barrier\n"
    "ds_read_b128 v[168:171], v30 offset:0\n"
    "v_mfma_f32_16x16x32_bf16 v[72:75], v[140:143], a[144:147], v[72:75]\n"
    "v_mfma_f32_16x16x32_bf16 v[76:79], v[140:143], a[148:151], v[76:79]\n"
    "ds_read_b128 v[172:175], v30 offset:1024\n"
    "v_mfma_f32_16x16x32_bf16 v[80:83], v[144:147], a[144:147], v[80:83]\n"
    "buffer_load_dwordx4 v23, s[48:51], s66 offen offset:2048 sc1 lds\n"
    "v_mfma_f32_16x16x32_bf16 v[84:87], v[144:147], a[148:151], v[84:87]\n"
    "ds_read_b128 v[176:179], v30 offset:2048\n"
    "s_add_u32 m0, s59, 131072\n"
    "s_waitcnt lgkmcnt(6)\n"
    "v_mfma_f32_16x16x32_bf16 v[88:91], v[148:151], a[144:147], v[88:91]\n"
    "s_add_u32 s67, s69, 20\n"
    "s_and_b32 s67, s67, 31\n"
    "s_mul_i32 s66, s67, 3072\n"
    "v_mfma_f32_16x16x32_bf16 v[92:95], v[148:151], a[148:151], v[92:95]\n"
    "ds_read_b128 v[180:183], v30 offset:3072\n"
    "s_add_u32 s66, s66, s68\n"
    "s_add_u32 s66, s66, 131072\n"
    "v_mfma_f32_16x16x32_bf16 v[64:67], v[152:155], a[152:155], v[64:67]\n"
    "buffer_load_dwordx4 v23, s[48:51], s66 offen offset:0 sc1 lds\n"
    "v_mfma_f32_16x16x32_bf16 v[68:71], v[152:155], a[156:159], v[68:71]\n"
    "ds_read_b128 v[136:139], v30 offset:4096\n"
    "v_mfma_f32_16x16x32_bf16 v[72:75], v[156:159], a[152:155], v[72:75]\n"
    "v_mfma_f32_16x16x32_bf16 v[76:79], v[156:159], a[156:159], v[76:79]\n"
    "ds_read_b128 v[140:143], v30 offset:5120\n"
    "s_waitcnt lgkmcnt(5)\n"
    "v_mfma_f32_16x16x32_bf16 v[80:83], v[160:163], a[152:155], v[80:83]\n"
    "buffer_load_dwordx4 v23, s[48:51], s66 offen offset:1024 sc1 lds\n"
    "v_mfma_f32_16x16x32_bf16 v[84:87], v[160:163], a[156:159], v[84:87]\n"
    "ds_read_b128 v[144:147], v30 offset:6144\n"
    "v_mfma_f32_16x16x32_bf16 v[88:91], v[164:167], a[152:155], v[88:91]\n"
    "v_mfma_f32_16x16x32_bf16 v[92:95], v[164:167], a[156:159], v[92:95]\n"
    "ds_read_b128 v[148:151], v30 offset:7168\n"
    "v_mfma_f32_16x16x32_bf16 v[64:67], v[168:171], a[160:163], v[64:67]\n"
    "v_mfma_f32_16x16x32_bf16 v[68:71], v[168:171], a[164:167], v[68:71]\n"
    "ds_read_b128 v[152:155], v30 offset:8192\n"
    "s_waitcnt lgkmcnt(5)\n"
    "v_mfma_f32_16x16x32_bf16 v[72:75], v[172:175], a[160:163], v[72:75]\n"
    "buffer_load_dwordx4 v23, s[48:51], s66 offen offset:2048 sc1 lds\n"
    "v_mfma_f32_16x16x32_bf16 v[76:79], v[172:175], a[164:167], v[76:79]\n"
    "ds_read_b128 v[156:159], v30 offset:9216\n"
    "s_add_u32 m0, s59, 49152\n"
    "s_add_u32 s67, s69, 24\n"
    "v_mfma_f32_16x16x32_bf16 v[80:83], v[176:179], a[160:163], v[80:83]\n"
    "s_and_b32 s67, s67, 31\n"
    "s_mul_i32 s66, s67, 3072\n"
    "s_add_u32 s66, s66, s68\n"
    "v_mfma_f32_16x16x32_bf16 v[84:87], v[176:179], a[164:167], v[84:87]\n"
    "ds_read_b128 v[160:163], v30 offset:10240\n"
    "s_add_u32 s66, s66, 131072\n"
    "v_mfma_f32_16x16x32_bf16 v[88:91], v[180:183], a[160:163], v[88:91]\n"
    "buffer_load_dwordx4 v23, s[48:51], s66 offen offset:0 sc1 lds\n"
    "v_mfma_f32_16x16x32_bf16 v[92:95], v[180:183], a[164:167], v[92:95]\n"
    "ds_read_b128 v[164:167], v30 offset:11264\n"
    "s_waitcnt lgkmcnt(5)\n"
    "v_mfma_f32_16x16x32_bf16 v[64:67], v[136:139], a[168:171], v[64:67]\n"
    "v_mfma_f32_16x16x32_bf16 v[68:71], v[136:139], a[172:175], v[68:71]\n"
    "ds_read_b128 v[168:171], v30 offset:12288\n"
    "v_mfma_f32_16x16x32_bf16 v[72:75], v[140:143], a[168:171], v[72:75]\n"
    "buffer_load_dwordx4 v23, s[48:51], s66 offen offset:1024 sc1 lds\n"
    "v_mfma_f32_16x16x32_bf16 v[76:79], v[140:143], a[172:175], v[76:79]\n"
    "ds_read_b128 v[172:175], v30 offset:13312\n"
    "v_mfma_f32_16x16x32_bf16 v[80:83], v[144:147], a[168:171], v[80:83]\n"
    "v_mfma_f32_16x16x32_bf16 v[84:87], v[144:147], a[172:175], v[84:87]\n"
    "ds_read_b128 v[176:179], v30 offset:14336\n"
    "s_waitcnt lgkmcnt(5)\n"
    "v_mfma_f32_16x16x32_bf16 v[88:91], v[148:151], a[168:171], v[88:91]\n"
    "buffer_load_dwordx4 v23, s[48:51], s66 offen offset:2048 sc1 lds\n"
    "v_mfma_f32_16x16x32_bf16 v[92:95], v[148:151], a[172:175], v[92:95]\n"
    "ds_read_b128 v[180:183], v30 offset:15360\n"
    "s_add_u32 m0, s59, 65536\n"
    "s_add_u32 s67, s69, 28\n"
    "v_mfma_f32_16x16x32_bf16 v[64:67], v[152:155], a[176:179], v[64:67]\n"
    "s_and_b32 s67, s67, 31\n"
    "s_mul_i32 s66, s67, 3072\n"
    "s_add_u32 s66, s66, s68\n"
    "v_mfma_f32_16x16x32_bf16 v[68:71], v[152:155], a[180:183], v[68:71]\n"
    "ds_read_b128 v[136:139], v30 offset:16384\n"
    "s_add_u32 s66, s66, 131072\n"
    "v_mfma_f32_16x16x32_bf16 v[72:75], v[156:159], a[176:179], v[72:75]\n"
    "v_mfma_f32_16x16x32_bf16 v[76:79], v[156:159], a[180:183], v[76:79]\n"
    "ds_read_b128 v[140:143], v30 offset:17408\n"
    "s_waitcnt lgkmcnt(5)\n"
    "v_mfma_f32_16x16x32_bf16 v[80:83], v[160:163], a[176:179], v[80:83]\n"
    "buffer_load_dwordx4 v23, s[48:51], s66 offen offset:0 sc1 lds\n"
    "v_mfma_f32_16x16x32_bf16 v[84:87], v[160:163], a[180:183], v[84:87]\n"
    "ds_read_b128 v[144:147], v30 offset:18432\n"
    "v_mfma_f32_16x16x32_bf16 v[88:91], v[164:167], a[176:179], v[88:91]\n"
    "v_mfma_f32_16x16x32_bf16 v[92:95], v[164:167], a[180:183], v[92:95]\n"
    "ds_read_b128 v[148:151], v30 offset:19456\n"
    "v_mfma_f32_16x16x32_bf16 v[64:67], v[168:171], a[184:187], v[64:67]\n"
    "buffer_load_dwordx4 v23, s[48:51], s66 offen offset:1024 sc1 lds\n"
    "v_mfma_f32_16x16x32_bf16 v[68:71], v[168:171], a[188:191], v[68:71]\n"
    "ds_read_b128 v[152:155], v30 offset:20480\n"
    "s_waitcnt lgkmcnt(5)\n"
    "v_mfma_f32_16x16x32_bf16 v[72:75], v[172:175], a[184:187], v[72:75]\n"
    "v_mfma_f32_16x16x32_bf16 v[76:79], v[172:175], a[188:191], v[76:79]\n"
    "ds_read_b128 v[156:159], v30 offset:21504\n"
    "v_mfma_f32_16x16x32_bf16 v[80:83], v[176:179], a[184:187], v[80:83]\n"
    "ds_read_b128 a[240:243], v28 offset:16384\n"
    "ds_read_b128 a[244:247], v28 offset:18432\n"
    "ds_read_b128 a[248:251], v29 offset:16384\n"
    "v_mfma_f32_16x16x32_bf16 v[84:87], v[176:179], a[188:191], v[84:87]\n"
    "ds_read_b128 v[160:163], v30 offset:22528\n"
    "ds_read_b128 a[252:255], v29 offset:18432\n"
    "v_mfma_f32_16x16x32_bf16 v[88:91], v[180:183], a[184:187], v[88:91]\n"
    "buffer_load_dwordx4 v23, s[48:51], s66 offen offset:2048 sc1 lds\n"
    "v_mfma_f32_16x16x32_bf16 v[92:95], v[180:183], a[188:191], v[92:95]\n"
    "ds_read_b128 v[164:167], v30 offset:23552\n"
    "s_waitcnt lgkmcnt(9)\n"
    "v_mfma_f32_16x16x32_bf16 v[64:67], v[136:139], a[192:195], v[64:67]\n"
    "v_mfma_f32_16x16x32_bf16 v[68:71], v[136:139], a[196:199], v[68:71]\n"
    "ds_read_b128 v[168:171], v30 offset:24576\n"
    "v_mfma_f32_16x16x32_bf16 v[72:75], v[140:143], a[192:195], v[72:75]\n"
    "v_mfma_f32_16x16x32_bf16 v[76:79], v[140:143], a[196:199], v[76:79]\n"
    "ds_read_b128 v[172:175], v30 offset:25600\n"
    "v_mfma_f32_16x16x32_bf16 v[80:83], v[144:147], a[192:195], v[80:83]\n"
    "v_mfma_f32_16x16x32_bf16 v[84:87], v[144:147], a[196:199], v[84:87]\n"
    "ds_read_b128 v[176:179], v30 offset:26624\n"
    "s_waitcnt lgkmcnt(9)\n"
    "v_mfma_f32_16x16x32_bf16 v[88:91], v[148:151], a[192:195], v[88:91]\n"
    "v_mfma_f32_16x16x32_bf16 v[92:95], v[148:151], a[196:199], v[92:95]\n"
    "ds_read_b128 v[180:183], v30 offset:27648\n"
    "v_mfma_f32_16x16x32_bf16 v[64:67], v[152:155], a[200:203], v[64:67]\n"
    "v_mfma_f32_16x16x32_bf16 v[68:71], v[152:155], a[204:207], v[68:71]\n"
    "ds_read_b128 v[136:139], v30 offset:28672\n"
    "v_mfma_f32_16x16x32_bf16 v[72:75], v[156:159], a[200:203], v[72:75]\n"
    "v_mfma_f32_16x16x32_bf16 v[76:79], v[156:159], a[204:207], v[76:79]\n"
    "ds_read_b128 v[140:143], v30 offset:29696\n"
    "s_waitcnt lgkmcnt(5)\n"
    "v_mfma_f32_16x16x32_bf16 v[80:83], v[160:163], a[200:203], v[80:83]\n"
    "s_add_u32 m0, s59, 81920\n"
    "s_add_u32 s67, s56, 0\n"
    "s_and_b32 s67, s67, 31\n"
    "v_mfma_f32_16x16x32_bf16 v[84:87], v[160:163], a[204:207], v[84:87]\n"
    "ds_read_b128 v[144:147], v30 offset:30720\n"
    "s_mul_i32 s66, s67, 2048\n"
    "s_add_u32 s66, s66, s68\n"
    "v_mfma_f32_16x16x32_bf16 v[88:91], v[164:167], a[200:203], v[88:91]\n"
    "s_add_u32 s66, s66, 229376\n"
    "v_mfma_f32_16x16x32_bf16 v[92:95], v[164:167], a[204:207], v[92:95]\n"
    "ds_read_b128 v[148:151], v30 offset:31744\n"
    "v_mfma_f32_16x16x32_bf16 v[64:67], v[168:171], a[208:211], v[64:67]\n"
    "buffer_load_dwordx4 v23, s[48:51], s66 offen offset:0 sc1 lds\n"
    "v_mfma_f32_16x16x32_bf16 v[68:71], v[168:171], a[212:215], v[68:71]\n"
    "s_waitcnt vmcnt(13)\n"
    "s_barrier\n"
    "ds_read_b128 v[152:155], v30 offset:32768\n"
    "s_waitcnt lgkmcnt(5)\n"
    "v_mfma_f32_16x16x32_bf16 v[72:75], v[172:175], a[208:211], v[72:75]\n"
    "buffer_load_dwordx4 v23, s[48:51], s66 offen offset:1024 sc1 lds\n"
    "v_mfma_f32_16x16x32_bf16 v[76:79], v[172:175], a[212:215], v[76:79]\n"
    "ds_read_b128 v[156:159], v30 offset:33792\n"
    "v_mfma_f32_16x16x32_bf16 v[80:83], v[176:179], a[208:211], v[80:83]\n"
    "buffer_load_dwordx4 v23, s[48:51], s66 offen offset:2048 sc1 lds\n"
    "v_mfma_f32_16x16x32_bf16 v[84:87], v[176:179], a[212:215], v[84:87]\n"
    "ds_read_b128 v[160:163], v30 offset:36864\n"
    "v_mfma_f32_16x16x32_bf16 v[88:91], v[180:183], a[208:211], v[88:91]\n"
    "v_mfma_f32_16x16x32_bf16 v[92:95], v[180:183], a[212:215], v[92:95]\n"
    "ds_read_b128 v[164:167], v30 offset:37888\n"
    "s_waitcnt lgkmcnt(5)\n"
    "v_mfma_f32_16x16x32_bf16 v[64:67], v[136:139], a[216:219], v[64:67]\n"
    "buffer_load_dwordx4 v23, s[48:51], s66 offen offset:3072 sc1 lds\n"
    "v_mfma_f32_16x16x32_bf16 v[68:71], v[136:139], a[220:223], v[68:71]\n"
    "ds_read_b128 v[168:171], v30 offset:40960\n"
    "s_add_u32 m0, s59, 147456\n"
    "s_add_u32 s67, s56, 8\n"
    "v_mfma_f32_16x16x32_bf16 v[72:75], v[140:143], a[216:219], v[72:75]\n"
    "buffer_store_dwordx4 v[60:63], v23, s[48:51], s13 offen offset:0 sc1\n"
    "v_mfma_f32_16x16x32_bf16 v[76:79], v[140:143], a[220:223], v[76:79]\n"
    "ds_read_b128 v[172:175], v30 offset:41984\n"
    "s_and_b32 s67, s67, 31\n"
    "s_mul_i32 s66, s67, 2048\n"
    "v_mfma_f32_16x16x32_bf16 v[80:83], v[144:147], a[216:219], v[80:83]\n"
    "buffer_store_dwordx4 v[60:63], v23, s[48:51], s13 offen offset:1024 sc1\n"
    "v_mfma_f32_16x16x32_bf16 v[84:87], v[144:147], a[220:223], v[84:87]\n"
    "ds_read_b128 v[176:179], v30 offset:45056\n"
    "s_add_u32 s66, s66, s68\n"
    "s_waitcnt lgkmcnt(5)\n"
    "v_mfma_f32_16x16x32_bf16 v[88:91], v[148:151], a[216:219], v[88:91]\n"
    "buffer_store_dwordx4 v[60:63], v23, s[48:51], s14 offen sc1\n"
    "v_mfma_f32_16x16x32_bf16 v[92:95], v[148:151], a[220:223], v[92:95]\n"
    "ds_read_b128 v[180:183], v30 offset:46080\n"
    "v_mfma_f32_16x16x32_bf16 v[64:67], v[152:155], a[224:227], v[64:67]\n"
    "s_add_u32 s66, s66, 229376\n"
    "v_mfma_f32_16x16x32_bf16 v[68:71], v[152:155], a[228:231], v[68:71]\n"
    "ds_read_b128 v[136:139], v30 offset:34816\n"
    "v_mfma_f32_16x16x32_bf16 v[72:75], v[156:159], a[224:227], v[72:75]\n"
    "buffer_load_dwordx4 v23, s[48:51], s66 offen offset:0 sc1 lds\n"
    "v_mfma_f32_16x16x32_bf16 v[76:79], v[156:159], a[228:231], v[76:79]\n"
    "ds_read_b128 v[140:143], v30 offset:35840\n"
    "s_waitcnt lgkmcnt(5)\n"
    "v_mfma_f32_16x16x32_bf16 v[64:67], v[160:163], a[232:235], v[64:67]\n"
    "v_mfma_f32_16x16x32_bf16 v[68:71], v[160:163], a[236:239], v[68:71]\n"
    "ds_read_b128 v[144:147], v30 offset:38912\n"
    "v_mfma_f32_16x16x32_bf16 v[72:75], v[164:167], a[232:235], v[72:75]\n"
    "v_mfma_f32_16x16x32_bf16 v[76:79], v[164:167], a[236:239], v[76:79]\n"
    "ds_read_b128 v[148:151], v30 offset:39936\n"
    "v_mfma_f32_16x16x32_bf16 v[64:67], v[168:171], a[240:243], v[64:67]\n"
    "buffer_load_dwordx4 v23, s[48:51], s66 offen offset:1024 sc1 lds\n"
    "v_mfma_f32_16x16x32_bf16 v[68:71], v[168:171], a[244:247], v[68:71]\n"
    "ds_read_b128 v[152:155], v30 offset:43008\n"
    "s_waitcnt lgkmcnt(5)\n"
    "v_mfma_f32_16x16x32_bf16 v[72:75], v[172:175], a[240:243], v[72:75]\n"
    "v_mfma_f32_16x16x32_bf16 v[76:79], v[172:175], a[244:247], v[76:79]\n"
    "ds_read_b128 v[156:159], v30 offset:44032\n"
    "v_mfma_f32_16x16x32_bf16 v[64:67], v[176:179], a[248:251], v[64:67]\n"
    "buffer_load_dwordx4 v23, s[48:51], s66 offen offset:2048 sc1 lds\n"
    "v_mfma_f32_16x16x32_bf16 v[68:71], v[176:179], a[252:255], v[68:71]\n"
    "ds_read_b128 v[160:163], v30 offset:47104\n"
    "v_mfma_f32_16x16x32_bf16 v[72:75], v[180:183], a[248:251], v[72:75]\n"
    "v_mfma_f32_16x16x32_bf16 v[76:79], v[180:183], a[252:255], v[76:79]\n"
    "ds_read_b128 v[164:167], v30 offset:48128\n"
    "s_waitcnt lgkmcnt(5)\n"
    "v_mfma_f32_16x16x32_bf16 v[80:83], v[136:139], a[224:227], v[80:83]\n"
    "buffer_load_dwordx4 v23, s[48:51], s66 offen offset:3072 sc1 lds\n"
    "v_mfma_f32_16x16x32_bf16 v[84:87], v[136:139], a[228:231], v[84:87]\n"
    "ds_read_b128 v[168:171], v31 offset:32768\n"
    "s_add_u32 m0, s59, 0\n"
    "s_add_u32 s67, s56, 16\n"
    "v_mfma_f32_16x16x32_bf16 v[88:91], v[140:143], a[224:227], v[88:91]\n"
    "s_and_b32 s67, s67, 31\n"
    "s_mul_i32 s66, s67, 2048\n"
    "s_add_u32 s66, s66, s68\n"
    "v_mfma_f32_16x16x32_bf16 v[92:95], v[140:143], a[228:231], v[92:95]\n"
    "ds_read_b128 v[172:175], v31 offset:33792\n"
    "s_add_u32 s66, s66, 229376\n"
    "v_mfma_f32_16x16x32_bf16 v[80:83], v[144:147], a[232:235], v[80:83]\n"
    "v_cvt_pk_bf16_f32 v200, v64, v65\n"
    "v_cvt_pk_bf16_f32 v201, v66, v67\n"
    "v_mfma_f32_16x16x32_bf16 v[84:87], v[144:147], a[236:239], v[84:87]\n"
    "ds_read_b128 v[176:179], v31 offset:34816\n"
    "s_waitcnt lgkmcnt(5)\n"
    "v_mfma_f32_16x16x32_bf16 v[88:91], v[148:151], a[232:235], v[88:91]\n"
    "v_cvt_pk_bf16_f32 v202, v72, v73\n"
    "v_cvt_pk_bf16_f32 v203, v74, v75\n"
    "v_mfma_f32_16x16x32_bf16 v[92:95], v[148:151], a[236:239], v[92:95]\n"
    "ds_read_b128 v[180:183], v31 offset:36864\n"
    "v_mfma_f32_16x16x32_bf16 v[80:83], v[152:155], a[240:243], v[80:83]\n"
    "buffer_store_dwordx4 v[200:203], v23, s[48:51], s11 offen offset:0 sc1\n"
    "v_mfma_f32_16x16x32_bf16 v[84:87], v[152:155], a[244:247], v[84:87]\n"
    "ds_read_b128 v[136:139], v31 offset:37888\n"
    "v_cvt_pk_bf16_f32 v204, v68, v69\n"
    "v_mfma_f32_16x16x32_bf16 v[88:91], v[156:159], a[240:243], v[88:91]\n"
    "v_cvt_pk_bf16_f32 v205, v70, v71\n"
    "v_cvt_pk_bf16_f32 v206, v76, v77\n"
    "v_mfma_f32_16x16x32_bf16 v[92:95], v[156:159], a[244:247], v[92:95]\n"
    "ds_read_b128 v[140:143], v31 offset:38912\n"
    "s_waitcnt lgkmcnt(5)\n"
    "v_mfma_f32_16x16x32_bf16 v[80:83], v[160:163], a[248:251], v[80:83]\n"
    "v_cvt_pk_bf16_f32 v207, v78, v79\n"
    "v_mfma_f32_16x16x32_bf16 v[84:87], v[160:163], a[252:255], v[84:87]\n"
    "ds_read_b128 v[144:147], v31 offset:40960\n"
    "v_mfma_f32_16x16x32_bf16 v[88:91], v[164:167], a[248:251], v[88:91]\n"
    "v_mfma_f32_16x16x32_bf16 v[92:95], v[164:167], a[252:255], v[92:95]\n"
    "ds_read_b128 v[148:151], v31 offset:41984\n"
    "v_mfma_f32_16x16x32_bf16 v[96:99], v[168:171], a[96:99], v[96:99]\n"
    "v_mfma_f32_16x16x32_bf16 v[100:103], v[168:171], a[100:103], v[100:103]\n"
    "ds_read_b128 v[152:155], v31 offset:43008\n"
    "s_waitcnt lgkmcnt(5)\n"
    "v_mfma_f32_16x16x32_bf16 v[104:107], v[172:175], a[96:99], v[104:107]\n"
    "v_mfma_f32_16x16x32_bf16 v[108:111], v[172:175], a[100:103], v[108:111]\n"
    "ds_read_b128 v[156:159], v31 offset:45056\n"
    "v_mfma_f32_16x16x32_bf16 v[112:115], v[176:179], a[96:99], v[112:115]\n"
    "v_mfma_f32_16x16x32_bf16 v[116:119], v[176:179], a[100:103], v[116:119]\n"
    "ds_read_b128 v[160:163], v31 offset:46080\n"
    "v_mfma_f32_16x16x32_bf16 v[96:99], v[180:183], a[104:107], v[96:99]\n"
    "v_mfma_f32_16x16x32_bf16 v[100:103], v[180:183], a[108:111], v[100:103]\n"
    "ds_read_b128 v[164:167], v31 offset:47104\n"
    "s_waitcnt lgkmcnt(5)\n"
    "v_mfma_f32_16x16x32_bf16 v[104:107], v[136:139], a[104:107], v[104:107]\n"
    "v_mfma_f32_16x16x32_bf16 v[108:111], v[136:139], a[108:111], v[108:111]\n"
    "s_waitcnt vmcnt(18)\n"
    "s_barrier\n"
    "ds_read_b128 v[168:171], v31 offset:49152\n"
    "v_mfma_f32_16x16x32_bf16 v[112:115], v[140:143], a[104:107], v[112:115]\n"
    "v_mfma_f32_16x16x32_bf16 v[116:119], v[140:143], a[108:111], v[116:119]\n"
    "ds_read_b128 v[172:175], v31 offset:50176\n"
    "v_mfma_f32_16x16x32_bf16 v[96:99], v[144:147], a[112:115], v[96:99]\n"
    "v_mfma_f32_16x16x32_bf16 v[100:103], v[144:147], a[116:119], v[100:103]\n"
    "ds_read_b128 v[176:179], v31 offset:51200\n"
    "s_waitcnt lgkmcnt(5)\n"
    "v_mfma_f32_16x16x32_bf16 v[104:107], v[148:151], a[112:115], v[104:107]\n"
    "v_mfma_f32_16x16x32_bf16 v[108:111], v[148:151], a[116:119], v[108:111]\n"
    "ds_read_b128 v[180:183], v31 offset:53248\n"
    "v_mfma_f32_16x16x32_bf16 v[112:115], v[152:155], a[112:115], v[112:115]\n"
    "v_mfma_f32_16x16x32_bf16 v[116:119], v[152:155], a[116:119], v[116:119]\n"
    "ds_read_b128 v[136:139], v31 offset:54272\n"
    "v_mfma_f32_16x16x32_bf16 v[96:99], v[156:159], a[120:123], v[96:99]\n"
    "v_mfma_f32_16x16x32_bf16 v[100:103], v[156:159], a[124:127], v[100:103]\n"
    "ds_read_b128 v[140:143], v31 offset:55296\n"
    "s_waitcnt lgkmcnt(5)\n"
    "v_mfma_f32_16x16x32_bf16 v[104:107], v[160:163], a[120:123], v[104:107]\n"
    "v_mfma_f32_16x16x32_bf16 v[108:111], v[160:163], a[124:127], v[108:111]\n"
    "ds_read_b128 v[144:147], v31 offset:57344\n"
    "v_mfma_f32_16x16x32_bf16 v[112:115], v[164:167], a[120:123], v[112:115]\n"
    "v_mfma_f32_16x16x32_bf16 v[116:119], v[164:167], a[124:127], v[116:119]\n"
    "ds_read_b128 v[148:151], v31 offset:58368\n"
    "v_mfma_f32_16x16x32_bf16 v[96:99], v[168:171], a[128:131], v[96:99]\n"
    "v_mfma_f32_16x16x32_bf16 v[100:103], v[168:171], a[132:135], v[100:103]\n"
    "ds_read_b128 v[152:155], v31 offset:59392\n"
    "s_waitcnt lgkmcnt(5)\n"
    "v_mfma_f32_16x16x32_bf16 v[104:107], v[172:175], a[128:131], v[104:107]\n"
    "v_mfma_f32_16x16x32_bf16 v[108:111], v[172:175], a[132:135], v[108:111]\n"
    "ds_read_b128 v[156:159], v31 offset:61440\n"
    "v_mfma_f32_16x16x32_bf16 v[112:115], v[176:179], a[128:131], v[112:115]\n"
    "v_mfma_f32_16x16x32_bf16 v[116:119], v[176:179], a[132:135], v[116:119]\n"
    "ds_read_b128 v[160:163], v31 offset:62464\n"
    "v_mfma_f32_16x16x32_bf16 v[96:99], v[180:183], a[136:139], v[96:99]\n"
    "buffer_store_dwordx4 v[204:207], v23, s[48:51], s11 offen offset:1024 sc1\n"
    "v_mfma_f32_16x16x32_bf16 v[100:103], v[180:183], a[140:143], v[100:103]\n"
    "ds_read_b128 v[164:167], v31 offset:63488\n"
    "s_waitcnt lgkmcnt(5)\n"
    "v_mfma_f32_16x16x32_bf16 v[104:107], v[136:139], a[136:139], v[104:107]\n"
    "v_cvt_pk_bf16_f32 v208, v80, v81\n"
    "v_cvt_pk_bf16_f32 v209, v82, v83\n"
    "v_mfma_f32_16x16x32_bf16 v[108:111], v[136:139], a[140:143], v[108:111]\n"
    "ds_read_b128 v[168:171], v32 offset:0\n"
    "v_cvt_pk_bf16_f32 v210, v88, v89\n"
    "v_mfma_f32_16x16x32_bf16 v[112:115], v[140:143], a[136:139], v[112:115]\n"
    "v_cvt_pk_bf16_f32 v211, v90, v91\n"
    "v_mfma_f32_16x16x32_bf16 v[116:119], v[140:143], a[140:143], v[116:119]\n"
    "ds_read_b128 v[172:175], v32 offset:1024\n"
    "v_mfma_f32_16x16x32_bf16 v[96:99], v[144:147], a[144:147], v[96:99]\n"
    "buffer_store_dwordx4 v[208:211], v23, s[48:51], s12 offen offset:0 sc1\n"
    "v_mfma_f32_16x16x32_bf16 v[100:103], v[144:147], a[148:151], v[100:103]\n"
    "ds_read_b128 v[176:179], v32 offset:2048\n"
    "s_waitcnt lgkmcnt(5)\n"
    "v_mfma_f32_16x16x32_bf16 v[104:107], v[148:151], a[144:147], v[104:107]\n"
    "v_cvt_pk_bf16_f32 v212, v84, v85\n"
    "v_cvt_pk_bf16_f32 v213, v86, v87\n"
    "v_mfma_f32_16x16x32_bf16 v[108:111], v[148:151], a[148:151], v[108:111]\n"
    "ds_read_b128 v[180:183], v32 offset:4096\n"
    "v_cvt_pk_bf16_f32 v214, v92, v93\n"
    "v_mfma_f32_16x16x32_bf16 v[112:115], v[152:155], a[144:147], v[112:115]\n"
    "v_cvt_pk_bf16_f32 v215, v94, v95\n"
    "v_mfma_f32_16x16x32_bf16 v[116:119], v[152:155], a[148:151], v[116:119]\n"
    "ds_read_b128 v[136:139], v32 offset:5120\n"
    "v_mfma_f32_16x16x32_bf16 v[96:99], v[156:159], a[152:155], v[96:99]\n"
    "buffer_store_dwordx4 v[212:215], v23, s[48:51], s12 offen offset:1024 sc1\n"
    "v_mfma_f32_16x16x32_bf16 v[100:103], v[156:159], a[156:159], v[100:103]\n"
    "ds_read_b128 v[140:143], v32 offset:6144\n"
    "s_waitcnt lgkmcnt(5)\n"
    "v_mfma_f32_16x16x32_bf16 v[104:107], v[160:163], a[152:155], v[104:107]\n"
    "v_mfma_f32_16x16x32_bf16 v[108:111], v[160:163], a[156:159], v[108:111]\n"
    "ds_read_b128 v[144:147], v32 offset:8192\n"
    "v_mfma_f32_16x16x32_bf16 v[112:115], v[164:167], a[152:155], v[112:115]\n"
    "buffer_load_dwordx4 v23, s[48:51], s66 offen offset:0 sc1 lds\n"
    "v_mfma_f32_16x16x32_bf16 v[116:119], v[164:167], a[156:159], v[116:119]\n"
    "ds_read_b128 v[148:151], v32 offset:9216\n"
    "v_mfma_f32_16x16x32_bf16 v[96:99], v[168:171], a[160:163], v[96:99]\n"
    "buffer_load_dwordx4 v23, s[48:51], s66 offen offset:1024 sc1 lds\n"
    "v_mfma_f32_16x16x32_bf16 v[100:103], v[168:171], a[164:167], v[100:103]\n"
    "ds_read_b128 v[152:155], v32 offset:10240\n"
    "s_waitcnt lgkmcnt(5)\n"
    "v_mfma_f32_16x16x32_bf16 v[104:107], v[172:175], a[160:163], v[104:107]\n"
    "v_mfma_f32_16x16x32_bf16 v[108:111], v[172:175], a[164:167], v[108:111]\n"
    "ds_read_b128 v[156:159], v32 offset:12288\n"
    "v_mfma_f32_16x16x32_bf16 v[112:115], v[176:179], a[160:163], v[112:115]\n"
    "buffer_load_dwordx4 v23, s[48:51], s66 offen offset:2048 sc1 lds\n"
    "v_mfma_f32_16x16x32_bf16 v[116:119], v[176:179], a[164:167], v[116:119]\n"
    "ds_read_b128 v[160:163], v32 offset:13312\n"
    "v_mfma_f32_16x16x32_bf16 v[96:99], v[180:183], a[168:171], v[96:99]\n"
    "v_mfma_f32_16x16x32_bf16 v[100:103], v[180:183], a[172:175], v[100:103]\n"
    "ds_read_b128 v[164:167], v32 offset:14336\n"
    "s_waitcnt lgkmcnt(5)\n"
    "v_mfma_f32_16x16x32_bf16 v[104:107], v[136:139], a[168:171], v[104:107]\n"
    "buffer_load_dwordx4 v23, s[48:51], s66 offen offset:3072 sc1 lds\n"
    "v_mfma_f32_16x16x32_bf16 v[108:111], v[136:139], a[172:175], v[108:111]\n"
    "s_waitcnt vmcnt(19)\n"
    "s_barrier\n"
    "ds_read_b128 v[168:171], v30 offset:49152\n"
    "v_mfma_f32_16x16x32_bf16 v[112:115], v[140:143], a[168:171], v[112:115]\n"
    "s_add_u32 m0, s59, 16384\n"
    "s_add_u32 s67, s56, 24\n"
    "s_and_b32 s67, s67, 31\n"
    "v_mfma_f32_16x16x32_bf16 v[116:119], v[140:143], a[172:175], v[116:119]\n"
    "ds_read_b128 v[172:175], v30 offset:50176\n"
    "s_mul_i32 s66, s67, 2048\n"
    "s_add_u32 s66, s66, s68\n"
    "v_mfma_f32_16x16x32_bf16 v[96:99], v[144:147], a[176:179], v[96:99]\n"
    "s_add_u32 s66, s66, 229376\n"
    "v_mfma_f32_16x16x32_bf16 v[100:103], v[144:147], a[180:183], v[100:103]\n"
    "ds_read_b128 v[176:179], v30 offset:51200\n"
    "s_waitcnt lgkmcnt(5)\n"
    "v_mfma_f32_16x16x32_bf16 v[104:107], v[148:151], a[176:179], v[104:107]\n"
    "buffer_load_dwordx4 v23, s[48:51], s66 offen offset:0 sc1 lds\n"
    "v_mfma_f32_16x16x32_bf16 v[108:111], v[148:151], a[180:183], v[108:111]\n"
    "ds_read_b128 v[180:183], v30 offset:53248\n"
    "v_mfma_f32_16x16x32_bf16 v[112:115], v[152:155], a[176:179], v[112:115]\n"
    "v_mfma_f32_16x16x32_bf16 v[116:119], v[152:155], a[180:183], v[116:119]\n"
    "ds_read_b128 v[136:139], v30 offset:54272\n"
    "v_mfma_f32_16x16x32_bf16 v[96:99], v[156:159], a[184:187], v[96:99]\n"
    "v_mfma_f32_16x16x32_bf16 v[100:103], v[156:159], a[188:191], v[100:103]\n"
    "ds_read_b128 v[140:143], v30 offset:55296\n"
    "s_waitcnt lgkmcnt(5)\n"
    "v_mfma_f32_16x16x32_bf16 v[104:107], v[160:163], a[184:187], v[104:107]\n"
    "v_mfma_f32_16x16x32_bf16 v[108:111], v[160:163], a[188:191], v[108:111]\n"
    "ds_read_b128 v[144:147], v30 offset:57344\n"
    "v_mfma_f32_16x16x32_bf16 v[112:115], v[164:167], a[184:187], v[112:115]\n"
    "v_mfma_f32_16x16x32_bf16 v[116:119], v[164:167], a[188:191], v[116:119]\n"
    "ds_read_b128 v[148:151], v30 offset:58368\n"
    "v_mfma_f32_16x16x32_bf16 v[96:99], v[168:171], a[192:195], v[96:99]\n"
    "v_mfma_f32_16x16x32_bf16 v[100:103], v[168:171], a[196:199], v[100:103]\n"
    "ds_read_b128 v[152:155], v30 offset:59392\n"
    "s_waitcnt lgkmcnt(5)\n"
    "v_mfma_f32_16x16x32_bf16 v[104:107], v[172:175], a[192:195], v[104:107]\n"
    "v_mfma_f32_16x16x32_bf16 v[108:111], v[172:175], a[196:199], v[108:111]\n"
    "ds_read_b128 v[156:159], v30 offset:61440\n"
    "v_mfma_f32_16x16x32_bf16 v[112:115], v[176:179], a[192:195], v[112:115]\n"
    "v_mfma_f32_16x16x32_bf16 v[116:119], v[176:179], a[196:199], v[116:119]\n"
    "ds_read_b128 v[160:163], v30 offset:62464\n"
    "v_mfma_f32_16x16x32_bf16 v[96:99], v[180:183], a[200:203], v[96:99]\n"
    "v_mfma_f32_16x16x32_bf16 v[100:103], v[180:183], a[204:207], v[100:103]\n"
    "ds_read_b128 v[164:167], v30 offset:63488\n"
    "s_waitcnt lgkmcnt(5)\n"
    "v_mfma_f32_16x16x32_bf16 v[104:107], v[136:139], a[200:203], v[104:107]\n"
    "v_mfma_f32_16x16x32_bf16 v[108:111], v[136:139], a[204:207], v[108:111]\n"
    "ds_read_b128 v[168:171], v31 offset:0\n"
    "v_mfma_f32_16x16x32_bf16 v[112:115], v[140:143], a[200:203], v[112:115]\n"
    "v_mfma_f32_16x16x32_bf16 v[116:119], v[140:143], a[204:207], v[116:119]\n"
    "ds_read_b128 v[172:175], v31 offset:1024\n"
    "v_mfma_f32_16x16x32_bf16 v[96:99], v[144:147], a[208:211], v[96:99]\n"
    "v_mfma_f32_16x16x32_bf16 v[100:103], v[144:147], a[212:215], v[100:103]\n"
    "ds_read_b128 v[176:179], v31 offset:4096\n"
    "s_waitcnt lgkmcnt(5)\n"
    "v_mfma_f32_16x16x32_bf16 v[104:107], v[148:151], a[208:211], v[104:107]\n"
    "v_mfma_f32_16x16x32_bf16 v[108:111], v[148:151], a[212:215], v[108:111]\n"
    "ds_read_b128 v[180:183], v31 offset:5120\n"
    "v_mfma_f32_16x16x32_bf16 v[112:115], v[152:155], a[208:211], v[112:115]\n"
    "v_mfma_f32_16x16x32_bf16 v[116:119], v[152:155], a[212:215], v[116:119]\n"
    "ds_read_b128 v[136:139], v31 offset:8192\n"
    "v_mfma_f32_16x16x32_bf16 v[96:99], v[156:159], a[216:219], v[96:99]\n"
    "v_mfma_f32_16x16x32_bf16 v[100:103], v[156:159], a[220:223], v[100:103]\n"
    "ds_read_b128 v[140:143], v31 offset:9216\n"
    "s_waitcnt lgkmcnt(5)\n"
    "v_mfma_f32_16x16x32_bf16 v[104:107], v[160:163], a[216:219], v[104:107]\n"
    "v_mfma_f32_16x16x32_bf16 v[108:111], v[160:163], a[220:223], v[108:111]\n"
    "ds_read_b128 v[144:147], v31 offset:12288\n"
    "v_mfma_f32_16x16x32_bf16 v[112:115], v[164:167], a[216:219], v[112:115]\n"
    "v_mfma_f32_16x16x32_bf16 v[116:119], v[164:167], a[220:223], v[116:119]\n"
    "ds_read_b128 v[148:151], v31 offset:13312\n"
    "v_mfma_f32_16x16x32_bf16 v[96:99], v[168:171], a[224:227], v[96:99]\n"
    "v_mfma_f32_16x16x32_bf16 v[100:103], v[168:171], a[228:231], v[100:103]\n"
    "ds_read_b128 v[152:155], v31 offset:2048\n"
    "s_waitcnt lgkmcnt(5)\n"
    "v_mfma_f32_16x16x32_bf16 v[104:107], v[172:175], a[224:227], v[104:107]\n"
    "v_mfma_f32_16x16x32_bf16 v[108:111], v[172:175], a[228:231], v[108:111]\n"
    "ds_read_b128 v[156:159], v31 offset:6144\n"
    "s_waitcnt vmcnt(5)\n"
    "s_mov_b64 exec, 1\n"
    "buffer_store_dwordx2 v[38:39], v45, s[48:51], s29 offen offset:0 sc1\n"
    "s_mov_b64 exec, -1\n"
    "v_mfma_f32_16x16x32_bf16 v[96:99], v[176:179], a[232:235], v[96:99]\n"
    "buffer_load_dwordx4 v23, s[48:51], s66 offen offset:1024 sc1 lds\n"
    "v_mfma_f32_16x16x32_bf16 v[100:103], v[176:179], a[236:239], v[100:103]\n"
    "ds_read_b128 v[160:163], v31 offset:10240\n"
    "v_mfma_f32_16x16x32_bf16 v[104:107], v[180:183], a[232:235], v[104:107]\n"
    "buffer_load_dwordx4 v23, s[48:51], s66 offen offset:2048 sc1 lds\n"
    "v_mfma_f32_16x16x32_bf16 v[108:111], v[180:183], a[236:239], v[108:111]\n"
    "ds_read_b128 v[164:167], v31 offset:14336\n"
    "s_waitcnt lgkmcnt(5)\n"
    "v_mfma_f32_16x16x32_bf16 v[96:99], v[136:139], a[240:243], v[96:99]\n"
    "v_mfma_f32_16x16x32_bf16 v[100:103], v[136:139], a[244:247], v[100:103]\n"
    "s_barrier\n"
    "ds_read_b128 v[168:171], v31 offset:16384\n"
    "v_mfma_f32_16x16x32_bf16 v[104:107], v[140:143], a[240:243], v[104:107]\n"
    "buffer_load_dwordx4 v23, s[48:51], s66 offen offset:3072 sc1 lds\n"
    "v_mfma_f32_16x16x32_bf16 v[108:111], v[140:143], a[244:247], v[108:111]\n"
    "ds_read_b128 v[172:175], v31 offset:17408\n"
    "v_mfma_f32_16x16x32_bf16 v[96:99], v[144:147], a[248:251], v[96:99]\n"
    "v_mfma_f32_16x16x32_bf16 v[100:103], v[144:147], a[252:255], v[100:103]\n"
    "ds_read_b128 v[176:179], v31 offset:18432\n"
    "s_waitcnt lgkmcnt(5)\n"
    "v_mfma_f32_16x16x32_bf16 v[104:107], v[148:151], a[248:251], v[104:107]\n"
    "v_mfma_f32_16x16x32_bf16 v[108:111], v[148:151], a[252:255], v[108:111]\n"
    "ds_read_b128 v[180:183], v31 offset:19456\n"
    "v_mfma_f32_16x16x32_bf16 v[112:115], v[152:155], a[224:227], v[112:115]\n"
    "v_mfma_f32_16x16x32_bf16 v[116:119], v[152:155], a[228:231], v[116:119]\n"
    "ds_read_b128 v[136:139], v31 offset:20480\n"
    "v_mfma_f32_16x16x32_bf16 v[112:115], v[156:159], a[232:235], v[112:115]\n"
    "v_mfma_f32_16x16x32_bf16 v[116:119], v[156:159], a[236:239], v[116:119]\n"
    "ds_read_b128 v[140:143], v31 offset:21504\n"
    "s_waitcnt lgkmcnt(5)\n"
    "v_mfma_f32_16x16x32_bf16 v[112:115], v[160:163], a[240:243], v[112:115]\n"
    "v_cvt_pk_bf16_f32 v216, v96, v97\n"
    "v_cvt_pk_bf16_f32 v217, v98, v99\n"
    "v_mfma_f32_16x16x32_bf16 v[116:119], v[160:163], a[244:247], v[116:119]\n"
    "ds_read_b128 v[144:147], v31 offset:22528\n"
    "v_cvt_pk_bf16_f32 v218, v104, v105\n"
    "v_mfma_f32_16x16x32_bf16 v[112:115], v[164:167], a[248:251], v[112:115]\n"
    "v_cvt_pk_bf16_f32 v219, v106, v107\n"
    "v_mfma_f32_16x16x32_bf16 v[116:119], v[164:167], a[252:255], v[116:119]\n"
    "ds_read_b128 v[148:151], v31 offset:23552\n"
    "v_mfma_f32_16x16x32_bf16 v[120:123], v[168:171], a[0:3], 0\n"
    "buffer_store_dwordx4 v[216:219], v23, s[48:51], s13 offen offset:0 sc1\n"
    "v_mfma_f32_16x16x32_bf16 v[124:127], v[168:171], a[4:7], 0\n"
    "ds_read_b128 v[152:155], v31 offset:24576\n"
    "s_waitcnt lgkmcnt(5)\n"
    "v_mfma_f32_16x16x32_bf16 v[128:131], v[172:175], a[0:3], 0\n"
    "v_cvt_pk_bf16_f32 v220, v100, v101\n"
    "v_cvt_pk_bf16_f32 v221, v102, v103\n"
    "v_mfma_f32_16x16x32_bf16 v[132:135], v[172:175], a[4:7], 0\n"
    "ds_read_b128 v[156:159], v31 offset:25600\n"
    "v_cvt_pk_bf16_f32 v222, v108, v109\n"
    "v_mfma_f32_16x16x32_bf16 v[120:123], v[176:179], a[8:11], v[120:123]\n"
    "v_cvt_pk_bf16_f32 v223, v110, v111\n"
    "v_mfma_f32_16x16x32_bf16 v[124:127], v[176:179], a[12:15], v[124:127]\n"
    "ds_read_b128 v[160:163], v31 offset:26624\n"
    "v_mfma_f32_16x16x32_bf16 v[128:131], v[180:183], a[8:11], v[128:131]\n"
    "buffer_store_dwordx4 v[220:223], v23, s[48:51], s13 offen offset:1024 sc1\n"
    "v_mfma_f32_16x16x32_bf16 v[132:135], v[180:183], a[12:15], v[132:135]\n"
    "ds_read_b128 v[164:167], v31 offset:27648\n"
    "s_waitcnt lgkmcnt(5)\n"
    "v_mfma_f32_16x16x32_bf16 v[120:123], v[136:139], a[16:19], v[120:123]\n"
    "v_cvt_pk_bf16_f32 v224, v112, v113\n"
    "v_cvt_pk_bf16_f32 v225, v114, v115\n"
    "v_mfma_f32_16x16x32_bf16 v[124:127], v[136:139], a[20:23], v[124:127]\n"
    "ds_read_b128 v[168:171], v31 offset:28672\n"
    "v_cvt_pk_bf16_f32 v226, v116, v117\n"
    "v_mfma_f32_16x16x32_bf16 v[128:131], v[140:143], a[16:19], v[128:131]\n"
    "v_cvt_pk_bf16_f32 v227, v118, v119\n"
    "v_mfma_f32_16x16x32_bf16 v[132:135], v[140:143], a[20:23], v[132:135]\n"
    "ds_read_b128 v[172:175], v31 offset:29696\n"
    "v_mfma_f32_16x16x32_bf16 v[120:123], v[144:147], a[24:27], v[120:123]\n"
    "buffer_store_dwordx4 v[224:227], v23, s[48:51], s14 offen sc1\n"
    "v_mfma_f32_16x16x32_bf16 v[124:127], v[144:147], a[28:31], v[124:127]\n"
    "ds_read_b128 v[176:179], v31 offset:30720\n"
    "s_waitcnt lgkmcnt(5)\n"
    "v_mfma_f32_16x16x32_bf16 v[128:131], v[148:151], a[24:27], v[128:131]\n"
    "v_mfma_f32_16x16x32_bf16 v[132:135], v[148:151], a[28:31], v[132:135]\n"
    "ds_read_b128 v[180:183], v31 offset:31744\n"
    "v_mfma_f32_16x16x32_bf16 v[120:123], v[152:155], a[32:35], v[120:123]\n"
    "v_mfma_f32_16x16x32_bf16 v[124:127], v[152:155], a[36:39], v[124:127]\n"
    "ds_read_b128 v[136:139], v32 offset:16384\n"
    "v_mfma_f32_16x16x32_bf16 v[128:131], v[156:159], a[32:35], v[128:131]\n"
    "v_mfma_f32_16x16x32_bf16 v[132:135], v[156:159], a[36:39], v[132:135]\n"
    "ds_read_b128 v[140:143], v32 offset:17408\n"
    "s_waitcnt lgkmcnt(5)\n"
    "v_mfma_f32_16x16x32_bf16 v[120:123], v[160:163], a[40:43], v[120:123]\n"
    "v_mfma_f32_16x16x32_bf16 v[124:127], v[160:163], a[44:47], v[124:127]\n"
    "ds_read_b128 v[144:147], v32 offset:18432\n"
    "v_mfma_f32_16x16x32_bf16 v[128:131], v[164:167], a[40:43], v[128:131]\n"
    "v_mfma_f32_16x16x32_bf16 v[132:135], v[164:167], a[44:47], v[132:135]\n"
    "ds_read_b128 v[148:151], v32 offset:19456\n"
    "v_mfma_f32_16x16x32_bf16 v[120:123], v[168:171], a[48:51], v[120:123]\n"
    "v_mfma_f32_16x16x32_bf16 v[124:127], v[168:171], a[52:55], v[124:127]\n"
    "ds_read_b128 v[152:155], v32 offset:20480\n"
    "s_waitcnt lgkmcnt(5)\n"
    "v_mfma_f32_16x16x32_bf16 v[128:131], v[172:175], a[48:51], v[128:131]\n"
    "v_mfma_f32_16x16x32_bf16 v[132:135], v[172:175], a[52:55], v[132:135]\n"
    "ds_read_b128 v[156:159], v32 offset:21504\n"
    "v_mfma_f32_16x16x32_bf16 v[120:123], v[176:179], a[56:59], v[120:123]\n"
    "v_mfma_f32_16x16x32_bf16 v[124:127], v[176:179], a[60:63], v[124:127]\n"
    "ds_read_b128 v[160:163], v32 offset:22528\n"
    "buffer_load_dwordx2 v[40:41], v44, s[48:51], s21 offen offset:0 sc1\n"
    "v_mfma_f32_16x16x32_bf16 v[128:131], v[180:183], a[56:59], v[128:131]\n"
    "v_mfma_f32_16x16x32_bf16 v[132:135], v[180:183], a[60:63], v[132:135]\n"
    "ds_read_b128 v[164:167], v32 offset:23552\n"
    "s_waitcnt lgkmcnt(5)\n"
    "v_mfma_f32_16x16x32_bf16 v[120:123], v[136:139], a[64:67], v[120:123]\n"
    "v_mfma_f32_16x16x32_bf16 v[124:127], v[136:139], a[68:71], v[124:127]\n"
    "ds_read_b128 v[168:171], v32 offset:24576\n"
    "v_mfma_f32_16x16x32_bf16 v[128:131], v[140:143], a[64:67], v[128:131]\n"
    "v_mfma_f32_16x16x32_bf16 v[132:135], v[140:143], a[68:71], v[132:135]\n"
    "ds_read_b128 v[172:175], v32 offset:25600\n"
    "v_mfma_f32_16x16x32_bf16 v[120:123], v[144:147], a[72:75], v[120:123]\n"
    "v_mfma_f32_16x16x32_bf16 v[124:127], v[144:147], a[76:79], v[124:127]\n"
    "ds_read_b128 v[176:179], v32 offset:26624\n"
    "s_waitcnt lgkmcnt(5)\n"
    "v_mfma_f32_16x16x32_bf16 v[128:131], v[148:151], a[72:75], v[128:131]\n"
    "v_mfma_f32_16x16x32_bf16 v[132:135], v[148:151], a[76:79], v[132:135]\n"
    "ds_read_b128 v[180:183], v32 offset:27648\n"
    "v_mfma_f32_16x16x32_bf16 v[120:123], v[152:155], a[80:83], v[120:123]\n"
    "v_mfma_f32_16x16x32_bf16 v[124:127], v[152:155], a[84:87], v[124:127]\n"
    "ds_read_b128 v[136:139], v32 offset:28672\n"
    "v_mfma_f32_16x16x32_bf16 v[128:131], v[156:159], a[80:83], v[128:131]\n"
    "v_mfma_f32_16x16x32_bf16 v[132:135], v[156:159], a[84:87], v[132:135]\n"
    "ds_read_b128 v[140:143], v32 offset:29696\n"
    "s_waitcnt lgkmcnt(5)\n"
    "v_mfma_f32_16x16x32_bf16 v[120:123], v[160:163], a[88:91], v[120:123]\n"
    "v_mfma_f32_16x16x32_bf16 v[124:127], v[160:163], a[92:95], v[124:127]\n"
    "ds_read_b128 v[144:147], v32 offset:30720\n"
    "v_mfma_f32_16x16x32_bf16 v[128:131], v[164:167], a[88:91], v[128:131]\n"
    "v_mfma_f32_16x16x32_bf16 v[132:135], v[164:167], a[92:95], v[132:135]\n"
    "ds_read_b128 v[148:151], v32 offset:31744\n"
    "v_mfma_f32_16x16x32_bf16 v[120:123], v[168:171], a[96:99], v[120:123]\n"
    "v_mfma_f32_16x16x32_bf16 v[124:127], v[168:171], a[100:103], v[124:127]\n"
    "s_waitcnt vmcnt(4)\n"
    "s_barrier\n"
    "ds_read_b128 v[152:155], v30 offset:0\n"
    "s_waitcnt lgkmcnt(5)\n"
    "v_mfma_f32_16x16x32_bf16 v[128:131], v[172:175], a[96:99], v[128:131]\n"
    "v_mfma_f32_16x16x32_bf16 v[132:135], v[172:175], a[100:103], v[132:135]\n"
    "ds_read_b128 v[156:159], v30 offset:1024\n"
    "v_mfma_f32_16x16x32_bf16 v[120:123], v[176:179], a[104:107], v[120:123]\n"
    "v_mfma_f32_16x16x32_bf16 v[124:127], v[176:179], a[108:111], v[124:127]\n"
    "ds_read_b128 v[160:163], v30 offset:2048\n"
    "v_mfma_f32_16x16x32_bf16 v[128:131], v[180:183], a[104:107], v[128:131]\n"
    "v_mfma_f32_16x16x32_bf16 v[132:135], v[180:183], a[108:111], v[132:135]\n"
    "ds_read_b128 v[164:167], v30 offset:3072\n"
    "s_waitcnt lgkmcnt(5)\n"
    "v_mfma_f32_16x16x32_bf16 v[120:123], v[136:139], a[112:115], v[120:123]\n"
    "v_mfma_f32_16x16x32_bf16 v[124:127], v[136:139], a[116:119], v[124:127]\n"
    "ds_read_b128 v[168:171], v30 offset:4096\n"
    "v_mfma_f32_16x16x32_bf16 v[128:131], v[140:143], a[112:115], v[128:131]\n"
    "v_mfma_f32_16x16x32_bf16 v[132:135], v[140:143], a[116:119], v[132:135]\n"
    "ds_read_b128 v[172:175], v30 offset:5120\n"
    "v_mfma_f32_16x16x32_bf16 v[120:123], v[144:147], a[120:123], v[120:123]\n"
    "v_mfma_f32_16x16x32_bf16 v[124:127], v[144:147], a[124:127], v[124:127]\n"
    "ds_read_b128 v[176:179], v30 offset:6144\n"
    "s_waitcnt lgkmcnt(5)\n"
    "v_mfma_f32_16x16x32_bf16 v[128:131], v[148:151], a[120:123], v[128:131]\n"
    "v_mfma_f32_16x16x32_bf16 v[132:135], v[148:151], a[124:127], v[132:135]\n"
    "ds_read_b128 v[180:183], v30 offset:7168\n"
    "s_waitcnt vmcnt(0)\n"
    "v_cmp_ne_u64 vcc, v[40:41], v[38:39]\n"
    "s_cbranch_vccnz L15_%=\n"
    "L14_%=:\n"
    "v_mfma_f32_16x16x32_bf16 v[120:123], v[152:155], a[128:131], v[120:123]\n"
    "buffer_load_dwordx4 v[64:67], v23, s[48:51], s15 offen offset:0 sc1\n"
    "v_mfma_f32_16x16x32_bf16 v[124:127], v[152:155], a[132:135], v[124:127]\n"
    "ds_read_b128 v[136:139], v30 offset:8192\n"
    "v_mfma_f32_16x16x32_bf16 v[128:131], v[156:159], a[128:131], v[128:131]\n"
    "buffer_load_dwordx4 v[68:71], v23, s[48:51], s15 offen offset:1024 sc1\n"
    "v_mfma_f32_16x16x32_bf16 v[132:135], v[156:159], a[132:135], v[132:135]\n"
    "ds_read_b128 v[140:143], v30 offset:9216\n"
    "s_waitcnt lgkmcnt(5)\n"
    "v_mfma_f32_16x16x32_bf16 v[120:123], v[160:163], a[136:139], v[120:123]\n"
    "buffer_load_dwordx4 v[80:83], v23, s[48:51], s26 offen offset:0 sc1\n"
    "v_mfma_f32_16x16x32_bf16 v[124:127], v[160:163], a[140:143], v[124:127]\n"
    "ds_read_b128 v[144:147], v30 offset:10240\n"
    "v_mfma_f32_16x16x32_bf16 v[128:131], v[164:167], a[136:139], v[128:131]\n"
    "buffer_load_dwordx4 v[84:87], v23, s[48:51], s26 offen offset:1024 sc1\n"
    "v_mfma_f32_16x16x32_bf16 v[132:135], v[164:167], a[140:143], v[132:135]\n"
    "ds_read_b128 v[148:151], v30 offset:11264\n"
    "v_mfma_f32_16x16x32_bf16 v[120:123], v[168:171], a[144:147], v[120:123]\n"
    "buffer_load_dwordx4 v[232:235], v23, s[48:51], s27 offen offset:0 sc1\n"
    "v_mfma_f32_16x16x32_bf16 v[124:127], v[168:171], a[148:151], v[124:127]\n"
    "ds_read_b128 v[152:155], v30 offset:12288\n"
    "s_waitcnt lgkmcnt(5)\n"
    "v_mfma_f32_16x16x32_bf16 v[128:131], v[172:175], a[144:147], v[128:131]\n"
    "buffer_load_dwordx4 v[236:239], v23, s[48:51], s27 offen offset:1024 sc1\n"
    "v_mfma_f32_16x16x32_bf16 v[132:135], v[172:175], a[148:151], v[132:135]\n"
    "ds_read_b128 v[156:159], v30 offset:13312\n"
    "v_mfma_f32_16x16x32_bf16 v[120:123], v[176:179], a[152:155], v[120:123]\n"
    "s_mov_b64 exec, s[30:31]\n"
    "buffer_load_dwordx4 v[240:243], v23, s[48:51], s15 offen offset:2048 sc1\n"
    "s_mov_b64 exec, -1\n"
    "v_mfma_f32_16x16x32_bf16 v[124:127], v[176:179], a[156:159], v[124:127]\n"
    "ds_read_b128 v[160:163], v30 offset:14336\n"
    "v_mfma_f32_16x16x32_bf16 v[128:131], v[180:183], a[152:155], v[128:131]\n"
    "s_mov_b64 exec, s[30:31]\n"
    "buffer_load_dwordx4 v[244:247], v23, s[48:51], s26 offen offset:2048 sc1\n"
    "s_mov_b64 exec, -1\n"
    "v_mfma_f32_16x16x32_bf16 v[132:135], v[180:183], a[156:159], v[132:135]\n"
    "ds_read_b128 v[164:167], v30 offset:15360\n"
    "s_waitcnt lgkmcnt(5)\n"
    "v_mfma_f32_16x16x32_bf16 v[120:123], v[136:139], a[160:163], v[120:123]\n"
    "s_mov_b64 exec, s[30:31]\n"
    "buffer_load_dwordx4 v[248:251], v23, s[48:51], s27 offen offset:2048 sc1\n"
    "s_mov_b64 exec, -1\n"
    "v_mfma_f32_16x16x32_bf16 v[124:127], v[136:139], a[164:167], v[124:127]\n"
    "ds_read_b128 v[168:171], v30 offset:16384\n"
    "v_mfma_f32_16x16x32_bf16 v[128:131], v[140:143], a[160:163], v[128:131]\n"
    "v_mfma_f32_16x16x32_bf16 v[132:135], v[140:143], a[164:167], v[132:135]\n"
    "ds_read_b128 v[172:175], v30 offset:17408\n"
    "v_mfma_f32_16x16x32_bf16 v[120:123], v[144:147], a[168:171], v[120:123]\n"
    "v_mfma_f32_16x16x32_bf16 v[124:127], v[144:147], a[172:175], v[124:127]\n"
    "ds_read_b128 v[176:179], v30 offset:18432\n"
    "s_waitcnt lgkmcnt(5)\n"
    "v_mfma_f32_16x16x32_bf16 v[128:131], v[148:151], a[168:171], v[128:131]\n"
    "v_mfma_f32_16x16x32_bf16 v[132:135], v[148:151], a[172:175], v[132:135]\n"
    "ds_read_b128 v[180:183], v30 offset:19456\n"
    "v_mfma_f32_16x16x32_bf16 v[120:123], v[152:155], a[176:179], v[120:123]\n"
    "v_mfma_f32_16x16x32_bf16 v[124:127], v[152:155], a[180:183], v[124:127]\n"
    "ds_read_b128 v[136:139], v30 offset:20480\n"
    "v_mfma_f32_16x16x32_bf16 v[128:131], v[156:159], a[176:179], v[128:131]\n"
    "v_mfma_f32_16x16x32_bf16 v[132:135], v[156:159], a[180:183], v[132:135]\n"
    "ds_read_b128 v[140:143], v30 offset:21504\n"
    "s_waitcnt lgkmcnt(5)\n"
    "v_mfma_f32_16x16x32_bf16 v[120:123], v[160:163], a[184:187], v[120:123]\n"
    "v_mfma_f32_16x16x32_bf16 v[124:127], v[160:163], a[188:191], v[124:127]\n"
    "ds_read_b128 v[144:147], v30 offset:22528\n"
    "v_mfma_f32_16x16x32_bf16 v[128:131], v[164:167], a[184:187], v[128:131]\n"
    "v_mfma_f32_16x16x32_bf16 v[132:135], v[164:167], a[188:191], v[132:135]\n"
    "ds_read_b128 v[148:151], v30 offset:23552\n"
    "v_mfma_f32_16x16x32_bf16 v[120:123], v[168:171], a[192:195], v[120:123]\n"
    "v_mfma_f32_16x16x32_bf16 v[124:127], v[168:171], a[196:199], v[124:127]\n"
    "ds_read_b128 v[152:155], v30 offset:24576\n"
    "s_waitcnt lgkmcnt(5)\n"
    "v_mfma_f32_16x16x32_bf16 v[128:131], v[172:175], a[192:195], v[128:131]\n"
    "v_mfma_f32_16x16x32_bf16 v[132:135], v[172:175], a[196:199], v[132:135]\n"
    "ds_read_b128 v[156:159], v30 offset:25600\n"
    "v_mfma_f32_16x16x32_bf16 v[120:123], v[176:179], a[200:203], v[120:123]\n"
    "v_mfma_f32_16x16x32_bf16 v[124:127], v[176:179], a[204:207], v[124:127]\n"
    "ds_read_b128 v[160:163], v30 offset:26624\n"
    "v_mfma_f32_16x16x32_bf16 v[128:131], v[180:183], a[200:203], v[128:131]\n"
    "v_mfma_f32_16x16x32_bf16 v[132:135], v[180:183], a[204:207], v[132:135]\n"
    "ds_read_b128 v[164:167], v30 offset:27648\n"
    "s_waitcnt lgkmcnt(5)\n"
    "v_mfma_f32_16x16x32_bf16 v[120:123], v[136:139], a[208:211], v[120:123]\n"
    "v_mfma_f32_16x16x32_bf16 v[124:127], v[136:139], a[212:215], v[124:127]\n"
    "ds_read_b128 v[168:171], v30 offset:28672\n"
    "v_mfma_f32_16x16x32_bf16 v[128:131], v[140:143], a[208:211], v[128:131]\n"
    "v_mfma_f32_16x16x32_bf16 v[132:135], v[140:143], a[212:215], v[132:135]\n"
    "ds_read_b128 v[172:175], v30 offset:29696\n"
    "v_mfma_f32_16x16x32_bf16 v[120:123], v[144:147], a[216:219], v[120:123]\n"
    "v_mfma_f32_16x16x32_bf16 v[124:127], v[144:147], a[220:223], v[124:127]\n"
    "ds_read_b128 v[176:179], v30 offset:30720\n"
    "s_waitcnt lgkmcnt(5)\n"
    "v_mfma_f32_16x16x32_bf16 v[128:131], v[148:151], a[216:219], v[128:131]\n"
    "v_mfma_f32_16x16x32_bf16 v[132:135], v[148:151], a[220:223], v[132:135]\n"
    "ds_read_b128 v[180:183], v30 offset:31744\n"
    "v_mfma_f32_16x16x32_bf16 v[120:123], v[152:155], a[224:227], v[120:123]\n"
    "v_mfma_f32_16x16x32_bf16 v[124:127], v[152:155], a[228:231], v[124:127]\n"
    "v_mfma_f32_16x16x32_bf16 v[128:131], v[156:159], a[224:227], v[128:131]\n"
    "v_mfma_f32_16x16x32_bf16 v[132:135], v[156:159], a[228:231], v[132:135]\n"
    "s_waitcnt lgkmcnt(3)\n"
    "v_mfma_f32_16x16x32_bf16 v[120:123], v[160:163], a[232:235], v[120:123]\n"
    "v_mfma_f32_16x16x32_bf16 v[124:127], v[160:163], a[236:239], v[124:127]\n"
    "v_mfma_f32_16x16x32_bf16 v[128:131], v[164:167], a[232:235], v[128:131]\n"
    "v_mfma_f32_16x16x32_bf16 v[132:135], v[164:167], a[236:239], v[132:135]\n"
    "v_mfma_f32_16x16x32_bf16 v[120:123], v[168:171], a[240:243], v[120:123]\n"
    "v_mfma_f32_16x16x32_bf16 v[124:127], v[168:171], a[244:247], v[124:127]\n"
    "s_waitcnt lgkmcnt(0)\n"
    "v_mfma_f32_16x16x32_bf16 v[128:131], v[172:175], a[240:243], v[128:131]\n"
    "v_mfma_f32_16x16x32_bf16 v[132:135], v[172:175], a[244:247], v[132:135]\n"
    "v_mfma_f32_16x16x32_bf16 v[120:123], v[176:179], a[248:251], v[120:123]\n"
    "v_mfma_f32_16x16x32_bf16 v[124:127], v[176:179], a[252:255], v[124:127]\n"
    "v_mfma_f32_16x16x32_bf16 v[128:131], v[180:183], a[248:251], v[128:131]\n"
    "v_mfma_f32_16x16x32_bf16 v[132:135], v[180:183], a[252:255], v[132:135]\n"
    "s_waitcnt vmcnt(5)\n"
    "v_mfma_f32_16x16x32_bf16 v[120:123], v[48:51], v[64:67], v[120:123]\n"
    "v_mfma_f32_16x16x32_bf16 v[128:131], v[52:55], v[64:67], v[128:131]\n"
    "v_mfma_f32_16x16x32_bf16 v[124:127], v[48:51], v[68:71], v[124:127]\n"
    "v_mfma_f32_16x16x32_bf16 v[132:135], v[52:55], v[68:71], v[132:135]\n"
    "v_mfma_f32_16x16x32_bf16 v[120:123], v[184:187], v[80:83], v[120:123]\n"
    "v_mfma_f32_16x16x32_bf16 v[128:131], v[188:191], v[80:83], v[128:131]\n"
    "v_mfma_f32_16x16x32_bf16 v[124:127], v[184:187], v[84:87], v[124:127]\n"
    "v_mfma_f32_16x16x32_bf16 v[132:135], v[188:191], v[84:87], v[132:135]\n"
    "L10_%=:\n"
    "s_mov_b32 s57, 0x200000\n"
    "L20_%=:\n"
    "s_waitcnt vmcnt(0)\n"
    "v_max3_u32 v2, v240, v241, v242\n"
    "v_max3_u32 v3, v232, v233, v234\n"
    "v_max3_u32 v2, v2, v243, v244\n"
    "v_max3_u32 v2, v2, v245, v246\n"
    "v_max3_u32 v2, v2, v247, v248\n"
    "v_max3_u32 v2, v2, v249, v250\n"
    "v_max3_u32 v3, v3, v235, v236\n"
    "v_max_u32 v2, v2, v251\n"
    "v_max3_u32 v3, v3, v237, v238\n"
    "v_and_b32 v2, s31, v2\n"
    "v_max3_u32 v2, v2, v3, v239\n"
    "v_cmp_eq_u32 vcc, -1, v2\n"
    "s_cbranch_vccnz L18_%=\n"
    "L19_%=:\n"
    "s_cmp_eq_u32 s22, s25\n"
    "v_mfma_f32_16x16x32_bf16 v[120:123], v[192:195], v[232:235], v[120:123]\n"
    "v_mfma_f32_16x16x32_bf16 v[128:131], v[196:199], v[232:235], v[128:131]\n"
    "v_mfma_f32_16x16x32_bf16 v[124:127], v[192:195], v[236:239], v[124:127]\n"
    "v_mfma_f32_16x16x32_bf16 v[132:135], v[196:199], v[236:239], v[132:135]\n"
    "s_cbranch_scc0 L16_%=\n"
    "s_waitcnt vmcnt(0)\n"
    "v_mfma_f32_16x16x32_bf16 v[112:115], v[48:51], v[240:243], v[112:115]\n"
    "v_mfma_f32_16x16x32_bf16 v[116:119], v[52:55], v[240:243], v[116:119]\n"
    "v_mfma_f32_16x16x32_bf16 v[112:115], v[184:187], v[244:247], v[112:115]\n"
    "v_mfma_f32_16x16x32_bf16 v[116:119], v[188:191], v[244:247], v[116:119]\n"
    "v_mfma_f32_16x16x32_bf16 v[112:115], v[192:195], v[248:251], v[112:115]\n"
    "v_mfma_f32_16x16x32_bf16 v[116:119], v[196:199], v[248:251], v[116:119]\n"
    "L16_%=:\n"
    "s_nop 6\n"
    "v_cvt_pk_bf16_f32 v136, v120, v121\n"
    "v_cvt_pk_bf16_f32 v137, v122, v123\n"
    "v_cvt_pk_bf16_f32 v138, v128, v129\n"
    "v_cvt_pk_bf16_f32 v139, v130, v131\n"
    "v_xor_b32 v136, s58, v136\n"
    "v_xor_b32 v137, s58, v137\n"
    "v_xor_b32 v138, s58, v138\n"
    "v_xor_b32 v139, s58, v139\n"
    "buffer_store_dwordx4 v[136:139], v46, s[52:55], s16 offen sc1\n"
    "v_cvt_pk_bf16_f32 v140, v124, v125\n"
    "v_cvt_pk_bf16_f32 v141, v126, v127\n"
    "v_cvt_pk_bf16_f32 v142, v132, v133\n"
    "v_cvt_pk_bf16_f32 v143, v134, v135\n"
    "v_xor_b32 v140, s58, v140\n"
    "v_xor_b32 v141, s58, v141\n"
    "v_xor_b32 v142, s58, v142\n"
    "v_xor_b32 v143, s58, v143\n"
    "buffer_store_dwordx4 v[140:143], v46, s[52:55], s17 offen sc1\n"
    "s_cbranch_scc0 L17_%=\n"
    "s_add_u32 s29, s18, 18432\n"
    "v_cvt_pk_bf16_f32 v56, v112, v113\n"
    "v_cvt_pk_bf16_f32 v57, v114, v115\n"
    "v_cvt_pk_bf16_f32 v58, v116, v117\n"
    "v_cvt_pk_bf16_f32 v59, v118, v119\n"
    "v_xor_b32 v56, s58, v56\n"
    "v_xor_b32 v57, s58, v57\n"
    "v_xor_b32 v58, s58, v58\n"
    "v_xor_b32 v59, s58, v59\n"
    "buffer_store_dwordx2 v[56:57], v47, s[52:55], s18 offen offset:256 sc1\n"
    "buffer_store_dwordx2 v[58:59], v47, s[52:55], s29 offen offset:256 sc1\n"
    "s_or_b32 s26, s23, s25\n"
    "s_cmp_eq_u32 s26, 0\n"
    "s_cbranch_scc0 L17_%=\n"
    "s_mov_b64 exec, 1\n"
    "buffer_store_dwordx2 v[38:39], v45, s[48:51], s19 offen sc1\n"
    "s_mov_b64 exec, -1\n"
    "L17_%=:\n"
    "s_endpgm\n"
    "L7_%=:\n"
    "s_mov_b32 s57, 0x200000\n"
    "L21_%=:\n"
    "buffer_load_dwordx2 v[40:41], v42, s[48:51], 0 offen offset:0 sc1\n"
    "s_waitcnt vmcnt(0)\n"
    "s_sub_u32 s57, s57, 1\n"
    "s_cbranch_scc1 L6_%=\n"
    "v_cmp_ne_u64 vcc, v[40:41], v[38:39]\n"
    "s_cbranch_vccnz L21_%=\n"
    "s_branch L6_%=\n"
    "L9_%=:\n"
    "s_mov_b32 s57, 0x200000\n"
    "L22_%=:\n"
    "buffer_load_dwordx2 v[40:41], v44, s[48:51], s21 offen offset:0 sc1\n"
    "s_waitcnt vmcnt(0)\n"
    "s_sub_u32 s57, s57, 1\n"
    "s_cbranch_scc1 L8_%=\n"
    "v_cmp_ne_u64 vcc, v[40:41], v[38:39]\n"
    "s_cbranch_vccnz L22_%=\n"
    "s_branch L8_%=\n"
    "L13_%=:\n"
    "s_mov_b32 s57, 0x200000\n"
    "L23_%=:\n"
    "buffer_load_dwordx2 v[40:41], v42, s[48:51], 0 offen offset:0 sc1\n"
    "s_waitcnt vmcnt(0)\n"
    "s_sub_u32 s57, s57, 1\n"
    "s_cbranch_scc1 L12_%=\n"
    "v_cmp_ne_u64 vcc, v[40:41], v[38:39]\n"
    "s_cbranch_vccnz L23_%=\n"
    "s_branch L12_%=\n"
    "L15_%=:\n"
    "s_mov_b32 s57, 0x200000\n"
    "L24_%=:\n"
    "buffer_load_dwordx2 v[40:41], v44, s[48:51], s21 offen offset:0 sc1\n"
    "s_waitcnt vmcnt(0)\n"
    "s_sub_u32 s57, s57, 1\n"
    "s_cbranch_scc1 L14_%=\n"
    "v_cmp_ne_u64 vcc, v[40:41], v[38:39]\n"
    "s_cbranch_vccnz L24_%=\n"
    "s_branch L14_%=\n"
    "L18_%=:\n"
    "s_sub_u32 s57, s57, 1\n"
    "s_cbranch_scc1 L19_%=\n"
    "buffer_load_dwordx4 v[232:235], v23, s[48:51], s27 offen offset:0 sc1\n"
    "buffer_load_dwordx4 v[236:239], v23, s[48:51], s27 offen offset:1024 sc1\n"
    "s_mov_b64 exec, s[30:31]\n"
    "buffer_load_dwordx4 v[240:243], v23, s[48:51], s15 offen offset:2048 sc1\n"
    "s_mov_b64 exec, s[30:31]\n"
    "buffer_load_dwordx4 v[244:247], v23, s[48:51], s26 offen offset:2048 sc1\n"
    "s_mov_b64 exec, s[30:31]\n"
    "buffer_load_dwordx4 v[248:251], v23, s[48:51], s27 offen offset:2048 sc1\n"
    "s_mov_b64 exec, -1\n"
    "s_branch L20_%=\n"
    "LEND_%=:\n"
    :: "{s[4:5]}"(__builtin_amdgcn_kernarg_segment_ptr()), "{s6}"(blockIdx.x), "{v0}"(threadIdx.x), "s"(lds)
    : "s7", "s8", "s9", "s10", "s11", "s12", "s13", "s14", "s15", "s16", "s17", "s18", "s19", "s20", "s21", "s22", "s23", "s24", "s25", "s26", "s27", "s28", "s29", "s30", "s31", "s33", "s34", "s35", "s36", "s37", "s38", "s39", "s40", "s41", "s42", "s43", "s44", "s45", "s46", "s47", "s48", "s49", "s50", "s51", "s52", "s53", "s54", "s55", "s56", "s57", "s58", "s59", "s60", "s61", "s62", "s63", "s64", "s65", "s66", "s67", "s68", "s69", "s70", "s71", "s72", "s73", "s74", "s75", "s76", "s77", "s78", "s79", "s80", "s81", "s82", "s83", "s84", "s85", "s86", "s87", "s88", "s89", "s90", "s91", "s92", "s93", "s94", "s95", "s96", "s97", "s98", "s99", "v1", "v2", "v3", "v4", "v5", "v6", "v7", "v8", "v9", "v10", "v11", "v12", "v13", "v14", "v15", "v16", "v17", "v18", "v19", "v20", "v21", "v22", "v23", "v24", "v25", "v26", "v27", "v28", "v29", "v30", "v31", "v32", "v33", "v34", "v35", "v36", "v37", "v38", "v39", "v40", "v41", "v42", "v43", "v44", "v45", "v46", "v47", "v48", "v49", "v50", "v51", "v52", "v53", "v54", "v55", "v56", "v57", "v58", "v59", "v60", "v61", "v62", "v63", "v64", "v65", "v66", "v67", "v68", "v69", "v70", "v71", "v72", "v73", "v74", "v75", "v76", "v77", "v78", "v79", "v80", "v81", "v82", "v83", "v84", "v85", "v86", "v87", "v88", "v89", "v90", "v91", "v92", "v93", "v94", "v95", "v96", "v97", "v98", "v99", "v100", "v101", "v102", "v103", "v104", "v105", "v106", "v107", "v108", "v109", "v110", "v111", "v112", "v113", "v114", "v115", "v116", "v117", "v118", "v119", "v120", "v121", "v122", "v123", "v124", "v125", "v126", "v127", "v128", "v129", "v130", "v131", "v132", "v133", "v134", "v135", "v136", "v137", "v138", "v139", "v140", "v141", "v142", "v143", "v144", "v145", "v146", "v147", "v148", "v149", "v150", "v151", "v152", "v153", "v154", "v155", "v156", "v157", "v158", "v159", "v160", "v161", "v162", "v163", "v164", "v165", "v166", "v167", "v168", "v169", "v170", "v171", "v172", "v173", "v174", "v175", "v176", "v177", "v178", "v179", "v180", "v181", "v182", "v183", "v184", "v185", "v186", "v187", "v188", "v189", "v190", "v191", "v192", "v193", "v194", "v195", "v196", "v197", "v198", "v199", "v200", "v201", "v202", "v203", "v204", "v205", "v206", "v207", "v208", "v209", "v210", "v211", "v212", "v213", "v214", "v215", "v216", "v217", "v218", "v219", "v220", "v221", "v222", "v223", "v224", "v225", "v226", "v227", "v228", "v229", "v230", "v231", "v232", "v233", "v234", "v235", "v236", "v237", "v238", "v239", "v240", "v241", "v242", "v243", "v244", "v245", "v246", "v247", "v248", "v249", "v250", "v251", "v252", "v253", "v254", "v255", "a0", "a1", "a2", "a3", "a4", "a5", "a6", "a7", "a8", "a9", "a10", "a11", "a12", "a13", "a14", "a15", "a16", "a17", "a18", "a19", "a20", "a21", "a22", "a23", "a24", "a25", "a26", "a27", "a28", "a29", "a30", "a31", "a32", "a33", "a34", "a35", "a36", "a37", "a38", "a39", "a40", "a41", "a42", "a43", "a44", "a45", "a46", "a47", "a48", "a49", "a50", "a51", "a52", "a53", "a54", "a55", "a56", "a57", "a58", "a59", "a60", "a61", "a62", "a63", "a64", "a65", "a66", "a67", "a68", "a69", "a70", "a71", "a72", "a73", "a74", "a75", "a76", "a77", "a78", "a79", "a80", "a81", "a82", "a83", "a84", "a85", "a86", "a87", "a88", "a89", "a90", "a91", "a92", "a93", "a94", "a95", "a96", "a97", "a98", "a99", "a100", "a101", "a102", "a103", "a104", "a105", "a106", "a107", "a108", "a109", "a110", "a111", "a112", "a113", "a114", "a115", "a116", "a117", "a118", "a119", "a120", "a121", "a122", "a123", "a124", "a125", "a126", "a127", "a128", "a129", "a130", "a131", "a132", "a133", "a134", "a135", "a136", "a137", "a138", "a139", "a140", "a141", "a142", "a143", "a144", "a145", "a146", "a147", "a148", "a149", "a150", "a151", "a152", "a153", "a154", "a155", "a156", "a157", "a158", "a159", "a160", "a161", "a162", "a163", "a164", "a165", "a166", "a167", "a168", "a169", "a170", "a171", "a172", "a173", "a174", "a175", "a176", "a177", "a178", "a179", "a180", "a181", "a182", "a183", "a184", "a185", "a186", "a187", "a188", "a189", "a190", "a191", "a192", "a193", "a194", "a195", "a196", "a197", "a198", "a199", "a200", "a201", "a202", "a203", "a204", "a205", "a206", "a207", "a208", "a209", "a210", "a211", "a212", "a213", "a214", "a215", "a216", "a217", "a218", "a219", "a220", "a221", "a222", "a223", "a224", "a225", "a226", "a227", "a228", "a229", "a230", "a231", "a232", "a233", "a234", "a235", "a236", "a237", "a238", "a239", "a240", "a241", "a242", "a243", "a244", "a245", "a246", "a247", "a248", "a249", "a250", "a251", "a252", "a253", "a254", "a255", "memory", "scc", "vcc");
}

extern "C" void kernel_launch(void* const* d_in, const int* in_sizes, int n_in,
                              void* d_out, int out_size, void* d_ws, size_t ws_size,
                              hipStream_t stream) {
  fnet_fused<<<256, 256, 0, stream>>>(d_in[0], d_in[1], d_in[2], d_in[3], d_in[4], d_out, d_ws);
}
```
